```python
import torch

def _softmax(x, axis=-1):
    return torch.softmax(x, dim=axis)
def _gelu(x):
    return torch.nn.functional.gelu(x, approximate="tanh")
def _silu(x):
    return torch.nn.functional.silu(x)
def _sigmoid(x):
    return torch.sigmoid(x)
def _layernorm(x, axis=-1, eps=1e-5):
    m = x.mean(dim=axis, keepdim=True)
    v = x.var(dim=axis, keepdim=True, unbiased=False)
    return (x - m) / torch.sqrt(v + eps)
def _scatter_add(tgt, idx, src, axis=0):
    return tgt.clone().index_add_(axis, idx, src)
def _segment_sum(src, seg, n):
    out = torch.zeros((n,) + src.shape[1:], dtype=src.dtype)
    return out.index_add_(0, seg, src)
def _pad(x, pw):
    flat = tuple(v for pair in reversed(pw) for v in pair)
    return torch.nn.functional.pad(x, flat)

kernel_name = 'gated_mix__ssm_conv_s4__ssm_conv_h3__prompt'

def setup_inputs():

    _w = (lambda a: (lambda t: t.to(torch.bfloat16) if t.is_floating_point() else t)(torch.as_tensor(a)))
    def _A_setup():
        B, L, D, N = 1, 2048, 512, 128
        A_re = -(lambda _x: torch.exp(torch.as_tensor(_x)))((lambda *s: torch.randn(*s, dtype=torch.bfloat16) * 6.4)(D, N))
        A_im = torch.pi * (lambda *s: torch.randn(*s, dtype=torch.bfloat16) * 0.05)(D, N)
        B_re = (lambda *s: torch.randn(*s, dtype=torch.bfloat16) * 0.1)(D, N)
        B_im = (lambda *s: torch.randn(*s, dtype=torch.bfloat16) * 0.1)(D, N)
        C_re = (lambda *s: torch.randn(*s, dtype=torch.bfloat16) * 0.1)(D, N)
        C_im = (lambda *s: torch.randn(*s, dtype=torch.bfloat16) * 0.1)(D, N)
        log_dt = (lambda *s: torch.randn(*s, dtype=torch.bfloat16) * 0.05)(D) * 0.5 - 4.0
        Dskip = (lambda *s: torch.randn(*s, dtype=torch.bfloat16) * 0.05)(D)
        return {
            "u": _w((lambda *s: torch.randn(*s, dtype=torch.bfloat16) * 0.05)(B, L, D)),
            "A_re": _w(A_re),
            "A_im": _w(A_im),
            "B_re": _w(B_re),
            "B_im": _w(B_im),
            "C_re": _w(C_re),
            "C_im": _w(C_im),
            "log_dt": _w(log_dt),
            "Dskip": _w(Dskip),
        }
    _A_in = _A_setup()

    def _B_setup():
        B, S, D = 1, 2048, 512
        ks = 4
        return {
            "q": _w((lambda *s: torch.randn(*s, dtype=torch.bfloat16) * 0.05)(B, S, D)),
            "k": _w((lambda *s: torch.randn(*s, dtype=torch.bfloat16) * 0.4)(B, S, D)),
            "v": _w((lambda *s: torch.randn(*s, dtype=torch.bfloat16) * 0.4)(B, S, D)),
            "shift_k": _w((lambda *s: torch.randn(*s, dtype=torch.bfloat16) * 0.4)(D, ks) * 0.5),
            "ssm_k": _w((lambda *s: torch.randn(*s, dtype=torch.bfloat16) * 0.4)(D, S) * (1.0 / S ** 0.5)),
            "ssm_D": _w((lambda *s: torch.randn(*s, dtype=torch.bfloat16) * 0.4)(D)),
        }
    _B_in = _B_setup()

    return {
        "A_u": _A_in["u"],
        "A_A_re": _A_in["A_re"],
        "A_A_im": _A_in["A_im"],
        "A_B_re": _A_in["B_re"],
        "A_B_im": _A_in["B_im"],
        "A_C_re": _A_in["C_re"],
        "A_C_im": _A_in["C_im"],
        "A_log_dt": _A_in["log_dt"],
        "A_Dskip": _A_in["Dskip"],
        "B_q": _B_in["q"],
        "B_k": _B_in["k"],
        "B_v": _B_in["v"],
        "B_shift_k": _B_in["shift_k"],
        "B_ssm_k": _B_in["ssm_k"],
        "B_ssm_D": _B_in["ssm_D"],
        "gate_w": _w((lambda *s: torch.randn(*s, dtype=torch.bfloat16) * 0.05)(512) * 16.0),
    }

def reference(A_u, A_A_re, A_A_im, A_B_re, A_B_im, A_C_re, A_C_im, A_log_dt, A_Dskip, B_q, B_k, B_v, B_shift_k, B_ssm_k, B_ssm_D, gate_w):
    def _A_ref(u, A_re, A_im, B_re, B_im, C_re, C_im, log_dt, Dskip):
        Bsz, L, D = u.shape
        dt = torch.exp(log_dt)[:, None]
        A = A_re + 1j * A_im
        Bc = B_re + 1j * B_im
        Cc = C_re + 1j * C_im
        logdA = dt * A
        dB = dt * Bc
        l = (lambda x, dt: x.to(dt))(torch.arange(L), A_re.dtype)
        P = torch.exp(logdA[:, :, None] * l[None, None, :])
        K = torch.einsum("dn,dnl,dn->dl", Cc, P, dB).real
        L2 = 2 * L
        Kp = _pad(K, ((0, 0), (0, L)))
        up = (lambda x, *d: x.permute(*d))(u, 0, 2, 1)
        up = _pad(up, ((0, 0), (0, 0), (0, L)))
        Kf = (lambda x, axes=(-2,-1): torch.fft.rfft2(x.float(), dim=axes))(Kp[:, None, :], axes=(-2, -1))
        Uf = (lambda x, axes=(-2,-1): torch.fft.rfft2(x.float(), dim=axes))(up[:, :, None, :], axes=(-2, -1))
        Yf = Uf * Kf[None, :, :, :]
        y = (lambda x, s, axes=(-2,-1): torch.fft.irfft2(x, s=s, dim=axes).to(torch.bfloat16))(Yf, (1, L2), axes=(-2, -1))[:, :, 0, :L]
        y = (lambda x, *d: x.permute(*d))(y, 0, 2, 1) + u * Dskip[None, None, :]
        return y

    def _B_ref(q, k, v, shift_k, ssm_k, ssm_D):
        B, S, D = q.shape
        ks = shift_k.shape[-1]
        k_pad = _pad(k, ((0, 0), (ks - 1, 0), (0, 0)))
        k_shift = torch.zeros_like(k)
        for t in range(ks):
            k_shift = k_shift + k_pad[:, t:t + S, :] * shift_k[:, ks - 1 - t][None, None, :]
        kv = k_shift * v
        L = 2 * S
        kv_pad = _pad(kv, ((0, 0), (0, S), (0, 0)))
        kf_pad = _pad(ssm_k, ((0, 0), (0, S)))
        kv_f = (lambda x, axes=(-2,-1): torch.fft.rfft2(x.float(), dim=axes))(kv_pad[:, None, :, :], axes=(1, 2))
        kf_f = (lambda x, axes=(-2,-1): torch.fft.rfft2(x.float(), dim=axes))(kf_pad[:, None, :], axes=(1, 2))
        kf_f = (lambda x, *d: x.permute(*d))(kf_f, 1, 2, 0)
        y_f = kv_f * kf_f[None, :, :, :]
        y = (lambda x, s, axes=(-2,-1): torch.fft.irfft2(x, s=s, dim=axes).to(torch.bfloat16))(y_f, (1, L), axes=(1, 2))[:, 0, :S, :]
        y = y + kv * ssm_D[None, None, :]
        return q * y

    x = A_u
    g = _sigmoid((x * gate_w).sum(-1, keepdims=True))
    a = _A_ref(x, A_A_re, A_A_im, A_B_re, A_B_im, A_C_re, A_C_im, A_log_dt, A_Dskip)
    b = _B_ref(x, B_k, B_v, B_shift_k, B_ssm_k, B_ssm_D)
    return g * a + (1 - g) * b
```

```cpp
#include <hip/hip_runtime.h>
#include <hip/hip_bf16.h>
#include <stdint.h>

typedef unsigned short u16;
typedef short s16x4 __attribute__((ext_vector_type(4)));
typedef short s16x8 __attribute__((ext_vector_type(8)));
typedef float f32x4 __attribute__((ext_vector_type(4)));
typedef unsigned u32x4 __attribute__((ext_vector_type(4)));
typedef unsigned u32x2 __attribute__((ext_vector_type(2)));
#define LDSP __attribute__((address_space(3)))

constexpr int L = 2048, D = 512, NM = 128;

constexpr size_t WS_XT = 0;
constexpr size_t WS_KA = WS_XT + 2ull * D * L * 2;
constexpr size_t WS_G = WS_KA + 1ull * D * L * 2;

__device__ __forceinline__ float bf_lo(unsigned x) { return __uint_as_float(x << 16); }
__device__ __forceinline__ float bf_hi(unsigned x) { return __uint_as_float(x & 0xffff0000u); }
__device__ __forceinline__ float bf2f(u16 x) { return __uint_as_float(((unsigned)x) << 16); }
__device__ __forceinline__ unsigned cvtpk(float lo, float hi) {
    typedef __bf16 bf16x2 __attribute__((ext_vector_type(2)));
    typedef float f32x2 __attribute__((ext_vector_type(2)));
    f32x2 v = {lo, hi};
    bf16x2 r = __builtin_convertvector(v, bf16x2);
    return __builtin_bit_cast(unsigned, r);
}
__device__ __forceinline__ float rbf(float x) { return bf_lo(cvtpk(x, x) & 0xffffu); }

struct cf { float re, im; };
__device__ __forceinline__ cf cmul(cf a, cf b) {
    cf r;
    r.re = a.re * b.re - a.im * b.im;
    r.im = a.re * b.im + a.im * b.re;
    return r;
}

constexpr int KG_ROWB = 256;
constexpr int KG_XROWS = 41, KG_YROWS = 16;
constexpr int KG_PLANE = (KG_XROWS + KG_YROWS) * KG_ROWB;
constexpr int KG_WAVE = 2 * KG_PLANE;
constexpr int PREP_LDS = 4 * KG_WAVE;

__device__ __forceinline__ void prep_transpose(int blk, const u16* __restrict__ u, const u16* __restrict__ kk,
                                               const u16* __restrict__ vv, const u16* __restrict__ shk,
                                               const u16* __restrict__ gw, u16* __restrict__ xt, float* __restrict__ gout,
                                               unsigned* lds) {
    const int tid = threadIdx.x;
    const int cb = blk & 7, tb = blk >> 3;
    const int cg = tid & 7, rg = tid >> 3;
    const int c0 = cb * 64 + cg * 8;
    const int t0 = tb * 64 + rg * 2;
    u32x4 ur[2], vr[2], kr[5], sw[4];
#pragma unroll
    for (int i = 0; i < 2; ++i) ur[i] = *(const u32x4*)(u + (size_t)(t0 + i) * D + c0);
#pragma unroll
    for (int i = 0; i < 5; ++i) {
        int t = t0 - 3 + i;
        u32x4 z = {0u, 0u, 0u, 0u};
        kr[i] = z;
        if (t >= 0) kr[i] = *(const u32x4*)(kk + (size_t)t * D + c0);
    }
#pragma unroll
    for (int i = 0; i < 2; ++i) vr[i] = *(const u32x4*)(vv + (size_t)(t0 + i) * D + c0);
#pragma unroll
    for (int i = 0; i < 4; ++i) sw[i] = *(const u32x4*)(shk + (size_t)c0 * 4 + i * 8);
    const int wv = tid >> 6, lane = tid & 63;
    const int gr0 = blk * 8 + wv * 2;
    u32x4 gx[2];
#pragma unroll
    for (int i = 0; i < 2; ++i) gx[i] = *(const u32x4*)(u + (size_t)(gr0 + i) * D + lane * 8);
    u32x4 gwv = *(const u32x4*)(gw + lane * 8);

    unsigned* lu = lds;
    unsigned* lkv = lds + 64 * 32;
    const int col = rg ^ (cg << 2);
#pragma unroll
    for (int j = 0; j < 8; ++j) {
        const int dw = j >> 1;
        float kf[5];
#pragma unroll
        for (int i = 0; i < 5; ++i) kf[i] = (j & 1) ? bf_hi(kr[i][dw]) : bf_lo(kr[i][dw]);
        const unsigned s01 = sw[j >> 1][(j & 1) * 2], s23 = sw[j >> 1][(j & 1) * 2 + 1];
        const float w0 = bf_lo(s01), w1 = bf_hi(s01), w2 = bf_lo(s23), w3 = bf_hi(s23);
        unsigned p = cvtpk(kf[0] * w3, kf[1] * w3);
        float sa = bf_lo(p), sb = bf_hi(p);
        p = cvtpk(kf[1] * w2, kf[2] * w2);
        p = cvtpk(sa + bf_lo(p), sb + bf_hi(p));
        sa = bf_lo(p); sb = bf_hi(p);
        p = cvtpk(kf[2] * w1, kf[3] * w1);
        p = cvtpk(sa + bf_lo(p), sb + bf_hi(p));
        sa = bf_lo(p); sb = bf_hi(p);
        p = cvtpk(kf[3] * w0, kf[4] * w0);
        p = cvtpk(sa + bf_lo(p), sb + bf_hi(p));
        sa = bf_lo(p); sb = bf_hi(p);
        const float va = (j & 1) ? bf_hi(vr[0][dw]) : bf_lo(vr[0][dw]);
        const float vb = (j & 1) ? bf_hi(vr[1][dw]) : bf_lo(vr[1][dw]);
        const unsigned kvp = cvtpk(sa * va, sb * vb);
        const unsigned up = (j & 1) ? ((ur[0][dw] >> 16) | (ur[1][dw] & 0xffff0000u))
                                    : ((ur[0][dw] & 0xffffu) | (ur[1][dw] << 16));
        const int ch = cg * 8 + j;
        lu[ch * 32 + col] = up;
        lkv[ch * 32 + col] = kvp;
    }
#pragma unroll
    for (int i = 0; i < 2; ++i) {
        float s = 0.f;
#pragma unroll
        for (int d = 0; d < 4; ++d) {
            unsigned p = cvtpk(bf_lo(gx[i][d]) * bf_lo(gwv[d]), bf_hi(gx[i][d]) * bf_hi(gwv[d]));
            s += bf_lo(p);
            s += bf_hi(p);
        }
#pragma unroll
        for (int o = 32; o >= 1; o >>= 1) s += __shfl_xor(s, o);
        if (lane == 0) {
            float sr = rbf(s);
            float g = 1.0f / (1.0f + expf(-sr));
            gout[gr0 + i] = rbf(g);
        }
    }
    __syncthreads();
    const int sg = tid & 7, chl = tid >> 3;
#pragma unroll
    for (int h = 0; h < 2; ++h) {
        const int ch = chl + 32 * h;
        const int off = ch * 32 + ((sg ^ (ch >> 3)) << 2);
        u32x4 a = *(const u32x4*)(lu + off);
        u32x4 b = *(const u32x4*)(lkv + off);
        const size_t go = (size_t)(cb * 64 + ch) * L + tb * 64 + sg * 8;
        *(u32x4*)(xt + go) = a;
        *(u32x4*)(xt + (size_t)D * L + go) = b;
    }
}

__device__ __forceinline__ cf cexp_p2(float lr, float fh, float fl, float l) {
    float mag = __builtin_amdgcn_exp2f(lr * l);
    float ph = __builtin_amdgcn_fractf(fh * l) + fl * l;
    cf r;
    r.re = mag * __builtin_amdgcn_cosf(ph);
    r.im = mag * __builtin_amdgcn_sinf(ph);
    return r;
}

__device__ __forceinline__ void kg_put(char* base, int row, int slot_dw, cf v, bool negim) {
    float im = negim ? -v.im : v.im;
    unsigned h = cvtpk(v.re, im);
    unsigned l = cvtpk(v.re - bf_lo(h), im - bf_hi(h));
    int unit = (slot_dw >> 2) ^ (row & 15);
    int off = row * KG_ROWB + unit * 16 + (slot_dw & 3) * 4;
    *(unsigned*)(base + off) = h;
    *(unsigned*)(base + KG_PLANE + off) = l;
}

__device__ __forceinline__ void prep_kgen(int blk, const u16* __restrict__ Are, const u16* __restrict__ Aim,
                                          const u16* __restrict__ Bre, const u16* __restrict__ Bim,
                                          const u16* __restrict__ Cre, const u16* __restrict__ Cim,
                                          const u16* __restrict__ logdt, u16* __restrict__ ka, char* lds) {
    const int tid = threadIdx.x, wv = tid >> 6, lane = tid & 63;
    const int cbase = (blk & 7) * 64 + (blk >> 3) * 2;
    const int c = cbase + (wv >> 1);
    const int n = (wv & 1) * 64 + lane;
    const int pi = c * NM + n;
    const float are = bf2f(Are[pi]), aim = bf2f(Aim[pi]);
    const float bre = bf2f(Bre[pi]), bim = bf2f(Bim[pi]);
    const float cre = bf2f(Cre[pi]), cim = bf2f(Cim[pi]);
    const float dt = rbf(expf(bf2f(logdt[c])));
    const float ar = dt * are, ai = dt * aim;
    cf dB = {dt * bre, dt * bim};
    cf Cc = {cre, cim};
    cf CB = cmul(Cc, dB);
    const float lr = ar * 1.4426950408889634f;
    const float I2PH = 0.15915494309189535f;
    const float I2PL = (float)(0.15915494309189535 - (double)0.15915494309189535f);
    const float fh = ai * I2PH;
    const float fl = __builtin_fmaf(ai, I2PH, -fh) + ai * I2PL;

    char* base = lds + wv * KG_WAVE;
    {
        cf y[16];
        y[0] = cf{1.f, 0.f};
        y[1] = cexp_p2(lr, fh, fl, 8.f);
        y[2] = cexp_p2(lr, fh, fl, 16.f);
        y[4] = cexp_p2(lr, fh, fl, 32.f);
        y[8] = cexp_p2(lr, fh, fl, 64.f);
        y[3] = cmul(y[1], y[2]);
        y[5] = cmul(y[1], y[4]);
        y[6] = cmul(y[2], y[4]);
        y[7] = cmul(y[3], y[4]);
#pragma unroll
        for (int i = 1; i < 8; ++i) y[8 + i] = cmul(y[i], y[8]);
#pragma unroll
        for (int i = 0; i < 16; ++i) kg_put(base, KG_XROWS + i, lane, y[i], true);
    }
    cf e[8];
    e[1] = cexp_p2(lr, fh, fl, 1.f);
    e[2] = cexp_p2(lr, fh, fl, 2.f);
    e[4] = cexp_p2(lr, fh, fl, 4.f);
    e[3] = cmul(e[1], e[2]);
    e[5] = cmul(e[1], e[4]);
    e[6] = cmul(e[2], e[4]);
    e[7] = cmul(e[3], e[4]);
    cf G[17];
    {
        cf p1 = cexp_p2(lr, fh, fl, 128.f), p2 = cexp_p2(lr, fh, fl, 256.f), p4 = cexp_p2(lr, fh, fl, 512.f);
        cf p8 = cexp_p2(lr, fh, fl, 1024.f), p16 = cexp_p2(lr, fh, fl, 2048.f);
        G[0] = CB;
        G[1] = cmul(G[0], p1);
        G[2] = cmul(G[0], p2);
        G[3] = cmul(G[1], p2);
#pragma unroll
        for (int i = 0; i < 4; ++i) G[4 + i] = cmul(G[i], p4);
#pragma unroll
        for (int i = 0; i < 8; ++i) G[8 + i] = cmul(G[i], p8);
        G[16] = cmul(G[0], p16);
    }
#pragma unroll
    for (int g = 0; g < 2; ++g) {
        kg_put(base, 8 * g, lane, G[g], false);
#pragma unroll
        for (int r = 1; r < 8; ++r) kg_put(base, 8 * g + r, lane, cmul(G[g], e[r]), false);
    }
#pragma unroll
    for (int g = 2; g < 4; ++g) {
        kg_put(base, 16 + 4 * (g - 2), lane, G[g], false);
#pragma unroll
        for (int r = 1; r < 4; ++r) kg_put(base, 16 + 4 * (g - 2) + r, lane, cmul(G[g], e[2 * r]), false);
    }
#pragma unroll
    for (int g = 4; g < 8; ++g) {
        kg_put(base, 24 + 2 * (g - 4), lane, G[g], false);
        kg_put(base, 24 + 2 * (g - 4) + 1, lane, cmul(G[g], e[4]), false);
    }
#pragma unroll
    for (int g = 8; g < 17; ++g) kg_put(base, 32 + (g - 8), lane, G[g], false);

    __builtin_amdgcn_s_waitcnt(0xC07F);
    __builtin_amdgcn_wave_barrier();
    const int fr = lane & 15, fg = lane >> 4;
    f32x4 acc[3];
#pragma unroll
    for (int t = 0; t < 3; ++t) acc[t] = f32x4{0.f, 0.f, 0.f, 0.f};
#pragma unroll
    for (int ks = 0; ks < 4; ++ks) {
        const int unit = ks * 4 + fg;
        const int yrow = KG_XROWS + fr;
        const int yoff = yrow * KG_ROWB + ((unit ^ (yrow & 15)) << 4);
        s16x8 yh = *(const s16x8*)(base + yoff);
        s16x8 yl = *(const s16x8*)(base + KG_PLANE + yoff);
#pragma unroll
        for (int t = 0; t < 3; ++t) {
            int row = t * 16 + fr;
            if (row > KG_XROWS - 1) row = KG_XROWS - 1;
            const int xoff = row * KG_ROWB + ((unit ^ (row & 15)) << 4);
            s16x8 xh = *(const s16x8*)(base + xoff);
            s16x8 xl = *(const s16x8*)(base + KG_PLANE + xoff);
            acc[t] = __builtin_amdgcn_mfma_f32_16x16x32_bf16(xl, yh, acc[t], 0, 0, 0);
            acc[t] = __builtin_amdgcn_mfma_f32_16x16x32_bf16(xh, yl, acc[t], 0, 0, 0);
            acc[t] = __builtin_amdgcn_mfma_f32_16x16x32_bf16(xh, yh, acc[t], 0, 0, 0);
        }
    }
    __builtin_amdgcn_s_waitcnt(0xC07F);
    __builtin_amdgcn_wave_barrier();
    float* kq = (float*)base;
#pragma unroll
    for (int t = 0; t < 3; ++t) {
#pragma unroll
        for (int j = 0; j < 4; ++j) {
            const int row = t * 16 + fg * 4 + j;
            int l0;
            if (t == 0) l0 = (row >> 3) * 128 + (row & 7);
            else if (t == 1) {
                int r = row - 16;
                l0 = (r < 8) ? (256 + (r >> 2) * 128 + (r & 3) * 2) : (512 + ((r - 8) >> 1) * 128 + ((r - 8) & 1) * 4);
            } else l0 = 1024 + (row - 32) * 128;
            const int l = l0 + 8 * fr;
            if (l <= 2048) kq[l] = acc[t][j];
        }
    }
    __syncthreads();
#pragma unroll
    for (int i = 0; i < 8; ++i) {
        const int idx = tid + 256 * i;
        const int ch = idx >> 10, lp = (idx & 1023) * 2;
        const float* k0 = (const float*)(lds + (2 * ch) * KG_WAVE);
        const float* k1 = (const float*)(lds + (2 * ch + 1) * KG_WAVE);
        const unsigned q = cvtpk((float)lp, (float)(lp + 1));
        const int qa = (int)bf_lo(q), qb = (int)bf_hi(q);
        const float va = k0[qa] + k1[qa], vb = k0[qb] + k1[qb];
        *(unsigned*)(ka + (size_t)(cbase + ch) * L + lp) = cvtpk(va, vb);
    }
}

__global__ __launch_bounds__(256) void prep_kernel(const u16* __restrict__ u, const u16* __restrict__ kk,
                                                   const u16* __restrict__ vv, const u16* __restrict__ shk,
                                                   const u16* __restrict__ gw, const u16* __restrict__ Are,
                                                   const u16* __restrict__ Aim, const u16* __restrict__ Bre,
                                                   const u16* __restrict__ Bim, const u16* __restrict__ Cre,
                                                   const u16* __restrict__ Cim, const u16* __restrict__ logdt,
                                                   char* __restrict__ ws) {
    extern __shared__ __attribute__((aligned(16))) char smem[];
    const int blk = blockIdx.x;
    if (blk < 256) {
        prep_kgen(blk, Are, Aim, Bre, Bim, Cre, Cim, logdt, (u16*)(ws + WS_KA), smem);
    } else {
        prep_transpose(blk - 256, u, kk, vv, shk, gw, (u16*)(ws + WS_XT), (float*)(ws + WS_G), (unsigned*)smem);
    }
}

constexpr int XPAD = 128, FPAD = 16;
constexpr int XS_B = (XPAD + L) * 2;
constexpr int FS_B = (FPAD + L) * 2;
constexpr int CC_B = XS_B + FS_B;
constexpr int Y_OFF = 4 * CC_B;
constexpr int CONV_LDS = Y_OFF + L * 8;

__device__ __forceinline__ s16x4 trread(const char LDSP* p) {
    return __builtin_amdgcn_ds_read_tr16_b64_v4i16((s16x4 LDSP*)p);
}
__device__ __forceinline__ s16x8 frag(const char LDSP* p0, const char LDSP* p1) {
    s16x4 a = trread(p0), b = trread(p1);
    return __builtin_shufflevector(a, b, 0, 1, 2, 3, 4, 5, 6, 7);
}
__device__ __forceinline__ float ror1(float x) {
    return __int_as_float(__builtin_amdgcn_update_dpp(0, __float_as_int(x), 0x13C, 0xf, 0xf, false));
}

__global__ __launch_bounds__(256) void conv_kernel(const char* __restrict__ ws, const u16* __restrict__ ssmk,
                                                   const u16* __restrict__ dskip, const u16* __restrict__ ssmd,
                                                   u16* __restrict__ out) {
    extern __shared__ __attribute__((aligned(16))) char smem[];
    const int tid = threadIdx.x, lane = tid & 63;
    const int wv = __builtin_amdgcn_readfirstlane(tid >> 6);
    const int blk = blockIdx.x;
    const int cbase = (blk & 7) * 64 + (blk >> 3) * 2;
    const int conv = wv & 1, c = cbase + (wv >> 1);
    const u16* xsrc = (const u16*)(ws + WS_XT) + (size_t)conv * D * L + (size_t)c * L;
    const u16* fsrc = (conv ? ssmk : (const u16*)(ws + WS_KA)) + (size_t)c * L;
    char* xs = smem + wv * CC_B;
    char* fs = xs + XS_B;
    u32x4 xv[4], fv[4];
#pragma unroll
    for (int i = 0; i < 4; ++i) {
        fv[i] = *(const u32x4*)(fsrc + i * 512 + lane * 8);
        xv[i] = *(const u32x4*)(xsrc + i * 512 + lane * 8);
    }
    {
        u32x4 z = {0u, 0u, 0u, 0u};
        if (lane < 16) *(u32x4*)(xs + lane * 16) = z;
        if (lane < 2) *(u32x4*)(fs + lane * 16) = z;
    }
#pragma unroll
    for (int i = 0; i < 4; ++i) {
        *(u32x4*)(fs + FPAD * 2 + i * 1024 + lane * 16) = fv[i];
        *(u32x4*)(xs + XPAD * 2 + i * 1024 + lane * 16) = xv[i];
    }
    __builtin_amdgcn_s_waitcnt(0xC07F);
    __builtin_amdgcn_wave_barrier();

    const int g = lane >> 4, q = (lane >> 2) & 3, p = lane & 3;
    const char LDSP* xb = (const char LDSP*)(xs) + 2 * (XPAD + 12 + 16 * p - 32 * g - 16 - 4 * q);
    const char LDSP* fb = (const char LDSP*)(fs) + 2 * (FPAD - 12 + 4 * p + 32 * g + 4 * q);
    u16* y4 = (u16*)(smem + Y_OFF);

    s16x8 xf[32], ff[16];
    float c1 = 0.f, c2 = 0.f, c3 = 0.f;
    const bool last = (lane == 63);
#pragma unroll
    for (int ph = 0; ph < 32; ++ph) {
        xf[ph] = frag(xb + 128 * ph + 32, xb + 128 * ph);
        if ((ph & 1) == 0) ff[ph >> 1] = frag(fb + 256 * (ph >> 1), fb + 256 * (ph >> 1) + 32);
        f32x4 acc = {0.f, 0.f, 0.f, 0.f};
#pragma unroll
        for (int kp = 0; kp <= (ph >> 1); ++kp)
            acc = __builtin_amdgcn_mfma_f32_16x16x32_bf16(xf[ph - 2 * kp], ff[kp], acc, 0, 0, 0);
        float s3 = acc[3];
        float s2 = acc[2] + ror1(last ? c3 : s3);
        float s1 = acc[1] + ror1(last ? c2 : s2);
        float y = acc[0] + ror1(last ? c1 : s1);
        c3 = s3; c2 = s2; c1 = s1;
        y4[(ph * 64 + lane) * 4 + wv] = (u16)(cvtpk(y, y) & 0xffffu);
    }
    __syncthreads();
    const float* gsrc = (const float*)(ws + WS_G);
    const float ds0 = bf2f(dskip[cbase]), ds1 = bf2f(dskip[cbase + 1]);
    const float sd0 = bf2f(ssmd[cbase]), sd1 = bf2f(ssmd[cbase + 1]);
    const u16* u0 = (const u16*)(smem + 0 * CC_B) + XPAD;
    const u16* kv0 = (const u16*)(smem + 1 * CC_B) + XPAD;
    const u16* u1 = (const u16*)(smem + 2 * CC_B) + XPAD;
    const u16* kv1 = (const u16*)(smem + 3 * CC_B) + XPAD;
#pragma unroll
    for (int i = 0; i < 8; ++i) {
        const int t = tid + 256 * i;
        const u32x2 yy = *(const u32x2*)(y4 + t * 4);
        const float gt = gsrc[t];
        const float omg = rbf(1.0f - gt);
        const float ua = bf2f(u0[t]), ub = bf2f(u1[t]), ka = bf2f(kv0[t]), kb = bf2f(kv1[t]);
        unsigned pp = cvtpk(ua * ds0, ub * ds1);
        unsigned a = cvtpk(bf_lo(yy[0]) + bf_lo(pp), bf_lo(yy[1]) + bf_hi(pp));
        pp = cvtpk(ka * sd0, kb * sd1);
        unsigned yb = cvtpk(bf_hi(yy[0]) + bf_lo(pp), bf_hi(yy[1]) + bf_hi(pp));
        unsigned b = cvtpk(ua * bf_lo(yb), ub * bf_hi(yb));
        unsigned ga = cvtpk(gt * bf_lo(a), gt * bf_hi(a));
        unsigned gb = cvtpk(omg * bf_lo(b), omg * bf_hi(b));
        unsigned o = cvtpk(bf_lo(ga) + bf_lo(gb), bf_hi(ga) + bf_hi(gb));
        *(unsigned*)(out + (size_t)t * D + cbase) = o;
    }
}

extern "C" void kernel_launch(void* const* d_in, const int* in_sizes, int n_in, void* d_out, int out_size, void* d_ws,
                              size_t ws_size, hipStream_t stream) {
    const u16* u = (const u16*)d_in[0];
    const u16* Are = (const u16*)d_in[1];
    const u16* Aim = (const u16*)d_in[2];
    const u16* Bre = (const u16*)d_in[3];
    const u16* Bim = (const u16*)d_in[4];
    const u16* Cre = (const u16*)d_in[5];
    const u16* Cim = (const u16*)d_in[6];
    const u16* logdt = (const u16*)d_in[7];
    const u16* dskip = (const u16*)d_in[8];
    const u16* kk = (const u16*)d_in[10];
    const u16* vv = (const u16*)d_in[11];
    const u16* shk = (const u16*)d_in[12];
    const u16* ssmk = (const u16*)d_in[13];
    const u16* ssmd = (const u16*)d_in[14];
    const u16* gw = (const u16*)d_in[15];
    char* ws = (char*)d_ws;
    hipFuncSetAttribute((const void*)prep_kernel, hipFuncAttributeMaxDynamicSharedMemorySize, PREP_LDS);
    prep_kernel<<<512, 256, PREP_LDS, stream>>>(u, kk, vv, shk, gw, Are, Aim, Bre, Bim, Cre, Cim, logdt, ws);
    conv_kernel<<<256, 256, CONV_LDS, stream>>>(ws, ssmk, dskip, ssmd, (u16*)d_out);
}
```

```cpp
#include <hip/hip_runtime.h>
#include <hip/hip_bf16.h>
#include <stdint.h>
#define STAMP(k, i) do {} while (0)
#define FUSED 1

typedef unsigned short u16;
typedef unsigned long long u64;
typedef short s16x4 __attribute__((ext_vector_type(4)));
typedef short s16x8 __attribute__((ext_vector_type(8)));
typedef float f32x4 __attribute__((ext_vector_type(4)));
typedef unsigned u32x4 __attribute__((ext_vector_type(4)));
typedef unsigned u32x2 __attribute__((ext_vector_type(2)));
#define LDSP __attribute__((address_space(3)))
#define GAS __attribute__((address_space(1)))
#define RLX_AGENT __ATOMIC_RELAXED, __HIP_MEMORY_SCOPE_AGENT
#define RLX_WG __ATOMIC_RELAXED, __HIP_MEMORY_SCOPE_WORKGROUP
#define SBAR() __builtin_amdgcn_sched_barrier(0)
#define CBAR() asm volatile("" ::: "memory")

constexpr int L = 2048, D = 512, NM = 128;

constexpr unsigned WS_XT = 0;
constexpr unsigned WS_G = WS_XT + 2u * D * L * 2;
constexpr unsigned WS_FLAG = WS_G + L * 4;
constexpr unsigned WS_GEN = WS_FLAG + 256 * 8;
constexpr unsigned WS_USED = WS_GEN + 256;
constexpr u64 TAG_INC = 0x9E3779B97F4A7C15ull;
constexpr unsigned SPIN_CAP = 1u << 22;

constexpr int XPAD = 128, FPAD = 16;
constexpr int XS_B = (XPAD + L) * 2;
constexpr int FS_B = (FPAD + L) * 2;
constexpr int CC_B = XS_B + FS_B;
constexpr int Y_OFF = 4 * CC_B;
constexpr int SYNC_OFF = Y_OFF + L * 8;
constexpr int KG_OFF = SYNC_OFF + 64;
constexpr int KG_ROWB = 256;
constexpr int KG_BUF = 16 * KG_ROWB * 2;
constexpr int KG_WAVE = 3 * KG_BUF + 64;
constexpr int LDS_BYTES = KG_OFF + 4 * KG_WAVE;
enum { SW_PROG = 0, SW_T1 = 4, SW_T2 = 5, SW_KP = 6, SW_KR = 8, SW_TAGR = 10, SW_TAG = 12 };

__device__ __forceinline__ float bf_lo(unsigned x) { return __uint_as_float(x << 16); }
__device__ __forceinline__ float bf_hi(unsigned x) { return __uint_as_float(x & 0xffff0000u); }
__device__ __forceinline__ float bf2f(u16 x) { return __uint_as_float(((unsigned)x) << 16); }
__device__ __forceinline__ unsigned cvtpk(float lo, float hi) {
    typedef __bf16 bf16x2 __attribute__((ext_vector_type(2)));
    typedef float f32x2 __attribute__((ext_vector_type(2)));
    f32x2 v = {lo, hi};
    bf16x2 r = __builtin_convertvector(v, bf16x2);
    return __builtin_bit_cast(unsigned, r);
}
__device__ __forceinline__ float rbf(float x) { return bf_lo(cvtpk(x, x)); }

__device__ __forceinline__ unsigned lds_ld(const unsigned LDSP* p) { return __hip_atomic_load(p, RLX_WG); }
__device__ __forceinline__ void lds_st(unsigned LDSP* p, unsigned v) { __hip_atomic_store(p, v, RLX_WG); }
__device__ __forceinline__ void lds_wait_ge(const unsigned LDSP* p, unsigned v) {
#pragma nounroll
    for (unsigned s = 0; s < SPIN_CAP; ++s) {
        if ((unsigned)__builtin_amdgcn_readfirstlane(lds_ld(p)) >= v) break;
        __builtin_amdgcn_s_sleep(1);
    }
    CBAR();
}
__device__ __forceinline__ unsigned lds_arrive(unsigned LDSP* p, int lane) {
    unsigned r = 0;
    CBAR();
    if (lane == 0) r = __hip_atomic_fetch_add(p, 1u, RLX_WG);
    return (unsigned)__builtin_amdgcn_readfirstlane(r);
}

template <int CTRL, int RMASK>
__device__ __forceinline__ float dpp_add(float x) {
    return x + __int_as_float(__builtin_amdgcn_update_dpp(0, __float_as_int(x), CTRL, RMASK, 0xf, true));
}
__device__ __forceinline__ float wave_sum63(float x) {
    x = dpp_add<0x111, 0xf>(x);
    x = dpp_add<0x112, 0xf>(x);
    x = dpp_add<0x114, 0xf>(x);
    x = dpp_add<0x118, 0xf>(x);
    x = dpp_add<0x142, 0xa>(x);
    x = dpp_add<0x143, 0xc>(x);
    return x;
}

struct cf { float re, im; };
__device__ __forceinline__ cf cmul(cf a, cf b) {
    cf r;
    r.re = a.re * b.re - a.im * b.im;
    r.im = a.re * b.im + a.im * b.re;
    return r;
}
__device__ __forceinline__ cf cexp_p2(float lr, float fh, float fl, float l) {
    float mag = __builtin_amdgcn_exp2f(lr * l);
    float ph = __builtin_amdgcn_fractf(fh * l) + fl * l;
    cf r;
    r.re = mag * __builtin_amdgcn_cosf(ph);
    r.im = mag * __builtin_amdgcn_sinf(ph);
    return r;
}
__device__ __forceinline__ void kg_put(char LDSP* buf, int row, int lane, cf v, bool negim) {
    float im = negim ? -v.im : v.im;
    unsigned h = cvtpk(v.re, im);
    unsigned l = cvtpk(v.re - bf_lo(h), im - bf_hi(h));
    int off = row * KG_ROWB + (((lane >> 2) ^ row) << 4) + (lane & 3) * 4;
    *(unsigned LDSP*)(buf + off) = h;
    *(unsigned LDSP*)(buf + 16 * KG_ROWB + off) = l;
}
__device__ __forceinline__ f32x4 kg_mfma(const char LDSP* xb, const s16x8* yh, const s16x8* yl, int fr, int fg, int nrows) {
    f32x4 acc = {0.f, 0.f, 0.f, 0.f};
    const int row = fr < nrows ? fr : nrows - 1;
#pragma unroll
    for (int ks = 0; ks < 4; ++ks) {
        const int xoff = row * KG_ROWB + (((ks * 4 + fg) ^ row) << 4);
        s16x8 xh = *(const s16x8 LDSP*)(xb + xoff);
        s16x8 xl = *(const s16x8 LDSP*)(xb + 16 * KG_ROWB + xoff);
        acc = __builtin_amdgcn_mfma_f32_16x16x32_bf16(xl, yh[ks], acc, 0, 0, 0);
        acc = __builtin_amdgcn_mfma_f32_16x16x32_bf16(xh, yl[ks], acc, 0, 0, 0);
        acc = __builtin_amdgcn_mfma_f32_16x16x32_bf16(xh, yh[ks], acc, 0, 0, 0);
    }
    return acc;
}

__device__ __forceinline__ s16x4 trread(const char LDSP* p) {
    return __builtin_amdgcn_ds_read_tr16_b64_v4i16((s16x4 LDSP*)p);
}
__device__ __forceinline__ s16x8 frag(const char LDSP* p0, const char LDSP* p1) {
    s16x4 a = trread(p0), b = trread(p1);
    return __builtin_shufflevector(a, b, 0, 1, 2, 3, 4, 5, 6, 7);
}
__device__ __forceinline__ float ror1(float x) {
    return __int_as_float(__builtin_amdgcn_update_dpp(0, __float_as_int(x), 0x13C, 0xf, 0xf, false));
}

template <int V> struct ic { static constexpr int value = V; };
template <int B, int E, class F>
__device__ __forceinline__ void static_for(F&& f) {
    if constexpr (B < E) { f(ic<B>{}); static_for<B + 1, E>(f); }
}

struct Args {
    const u16 *u, *kk, *vv, *shk, *gw, *Are, *Aim, *Bre, *Bim, *Cre, *Cim, *logdt, *dskip, *ssmk, *ssmd;
    u16* out;
    char* ws;
};

template <int MODE>
__global__ __launch_bounds__(512, 2) void fused_kernel(Args a) {
    extern __shared__ __attribute__((aligned(16))) char smem_g[];
    char LDSP* smem = (char LDSP*)smem_g;
    char* ws = a.ws;
    const int tid = threadIdx.x, lane = tid & 63;
    const int wv = __builtin_amdgcn_readfirstlane(tid >> 6);
    const int blk = blockIdx.x;
    const int cb = blk & 7, tb = blk >> 3;
    const int cbase = cb * 64 + tb * 2;
    unsigned LDSP* sw = (unsigned LDSP*)(smem + SYNC_OFF);
    const __amdgpu_buffer_rsrc_t rs = __builtin_amdgcn_make_buffer_rsrc(ws, 0, WS_USED, 0x00020000);
    STAMP(0, 0);
    if (tid < 16) sw[tid] = 0u;

    if (wv < 4) {
        const int conv = wv & 1, c = cbase + (wv >> 1);
        char LDSP* xs = smem + wv * CC_B;
        char LDSP* fs = xs + XS_B;
        u64 tag = 0;
        u32x4 fv[4];
        if (MODE != 2) {
            const int cg = tid & 7, rg = tid >> 3;
            const int c0 = cb * 64 + cg * 8;
            const int t0 = tb * 64 + rg * 2;
            u32x4 ur[2], vr[2], kr[5], sk[4];
#pragma unroll
            for (int i = 0; i < 5; ++i) {
                int t = t0 - 3 + i;
                u32x4 z = {0u, 0u, 0u, 0u};
                kr[i] = z;
                if (t >= 0) kr[i] = *(const u32x4*)(a.kk + (size_t)t * D + c0);
            }
#pragma unroll
            for (int i = 0; i < 2; ++i) vr[i] = *(const u32x4*)(a.vv + (size_t)(t0 + i) * D + c0);
#pragma unroll
            for (int i = 0; i < 2; ++i) ur[i] = *(const u32x4*)(a.u + (size_t)(t0 + i) * D + c0);
#pragma unroll
            for (int i = 0; i < 4; ++i) sk[i] = *(const u32x4*)(a.shk + (size_t)c0 * 4 + i * 8);
            const int gr0 = blk * 8 + wv * 2;
            u32x4 gx[2];
#pragma unroll
            for (int i = 0; i < 2; ++i) gx[i] = *(const u32x4*)(a.u + (size_t)(gr0 + i) * D + lane * 8);
            u32x4 gwv = *(const u32x4*)(a.gw + lane * 8);
            if (MODE == 0) tag = __hip_atomic_load((const GAS u64*)(ws + WS_GEN), RLX_AGENT) + TAG_INC;
            if (MODE == 0 && conv) {
#pragma unroll
                for (int i = 0; i < 4; ++i) fv[i] = *(const u32x4*)(a.ssmk + (size_t)c * L + i * 512 + lane * 8);
            }
            __builtin_amdgcn_s_barrier();
            CBAR();

            unsigned LDSP* lu = (unsigned LDSP*)(smem + Y_OFF);
            unsigned LDSP* lkv = lu + 64 * 32;
            const int col = rg ^ (cg << 2);
#pragma unroll
            for (int j = 0; j < 8; ++j) {
                const int dw = j >> 1;
                float kf[5];
#pragma unroll
                for (int i = 0; i < 5; ++i) kf[i] = (j & 1) ? bf_hi(kr[i][dw]) : bf_lo(kr[i][dw]);
                const unsigned s01 = sk[j >> 1][(j & 1) * 2], s23 = sk[j >> 1][(j & 1) * 2 + 1];
                const float w0 = bf_lo(s01), w1 = bf_hi(s01), w2 = bf_lo(s23), w3 = bf_hi(s23);
                unsigned p = cvtpk(kf[0] * w3, kf[1] * w3);
                float sa = bf_lo(p), sb = bf_hi(p);
                p = cvtpk(kf[1] * w2, kf[2] * w2);
                p = cvtpk(sa + bf_lo(p), sb + bf_hi(p));
                sa = bf_lo(p); sb = bf_hi(p);
                p = cvtpk(kf[2] * w1, kf[3] * w1);
                p = cvtpk(sa + bf_lo(p), sb + bf_hi(p));
                sa = bf_lo(p); sb = bf_hi(p);
                p = cvtpk(kf[3] * w0, kf[4] * w0);
                p = cvtpk(sa + bf_lo(p), sb + bf_hi(p));
                sa = bf_lo(p); sb = bf_hi(p);
                const float va = (j & 1) ? bf_hi(vr[0][dw]) : bf_lo(vr[0][dw]);
                const float vb = (j & 1) ? bf_hi(vr[1][dw]) : bf_lo(vr[1][dw]);
                const unsigned kvp = cvtpk(sa * va, sb * vb);
                const unsigned up = (j & 1) ? ((ur[0][dw] >> 16) | (ur[1][dw] & 0xffff0000u))
                                            : ((ur[0][dw] & 0xffffu) | (ur[1][dw] << 16));
                const int ch = cg * 8 + j;
                lu[ch * 32 + col] = up;
                lkv[ch * 32 + col] = kvp;
            }
            STAMP(0, 1);
            lds_arrive(sw + SW_T1, lane);
            {
                float s[2];
#pragma unroll
                for (int i = 0; i < 2; ++i) {
                    float acc = 0.f;
#pragma unroll
                    for (int d = 0; d < 4; ++d) {
                        unsigned p = cvtpk(bf_lo(gx[i][d]) * bf_lo(gwv[d]), bf_hi(gx[i][d]) * bf_hi(gwv[d]));
                        acc += bf_lo(p);
                        acc += bf_hi(p);
                    }
                    s[i] = wave_sum63(acc);
                }
                const unsigned sp = cvtpk(s[0], s[1]);
                const float e0 = __builtin_amdgcn_exp2f(-1.4426950408889634f * bf_lo(sp));
                const float e1 = __builtin_amdgcn_exp2f(-1.4426950408889634f * bf_hi(sp));
                const unsigned gp = cvtpk(__builtin_amdgcn_rcpf(1.0f + e0), __builtin_amdgcn_rcpf(1.0f + e1));
                if (lane == 63) {
                    u32x2 gv = {gp << 16, gp & 0xffff0000u};
                    __builtin_amdgcn_raw_buffer_store_b64(gv, rs, WS_G + gr0 * 4, 0, 16);
                }
            }
            lds_wait_ge(sw + SW_T1, 4);
            const int sg = tid & 7, chl = tid >> 3;
#pragma unroll
            for (int h = 0; h < 2; ++h) {
                const int ch = chl + 32 * h;
                const int off = ch * 32 + ((sg ^ (ch >> 3)) << 2);
                u32x4 va = *(const u32x4 LDSP*)(lu + off);
                u32x4 vb = *(const u32x4 LDSP*)(lkv + off);
                const unsigned go = ((unsigned)(cb * 64 + ch) * L + tb * 64 + sg * 8) * 2;
                __builtin_amdgcn_raw_buffer_store_b128(va, rs, WS_XT + go, 0, 16);
                __builtin_amdgcn_raw_buffer_store_b128(vb, rs, WS_XT + D * L * 2 + go, 0, 16);
            }
            STAMP(0, 2);
            asm volatile("s_waitcnt vmcnt(0)" ::: "memory");
            STAMP(0, 3);
            if (MODE == 0) {
                if (lds_arrive(sw + SW_T2, lane) == 3) {
                    if (lane == 0) {
                        __hip_atomic_store((GAS u64*)(ws + WS_FLAG) + cb * 32 + tb, tag, RLX_AGENT);
                        lds_st(sw + SW_TAG, (unsigned)tag);
                        lds_st(sw + SW_TAG + 1, (unsigned)(tag >> 32));
                        lds_st(sw + SW_TAGR, 1u);
                    }
                }
            }
        } else {
            if (conv) {
#pragma unroll
                for (int i = 0; i < 4; ++i) fv[i] = *(const u32x4*)(a.ssmk + (size_t)c * L + i * 512 + lane * 8);
            }
            __builtin_amdgcn_s_barrier();
            CBAR();
        }
        if (MODE == 1) return;
        {
            u32x4 z = {0u, 0u, 0u, 0u};
            if (lane < 16) *(u32x4 LDSP*)(xs + lane * 16) = z;
            if (lane < 2) *(u32x4 LDSP*)(fs + lane * 16) = z;
            if (conv) {
#pragma unroll
                for (int i = 0; i < 4; ++i) *(u32x4 LDSP*)(fs + FPAD * 2 + i * 1024 + lane * 16) = fv[i];
            }
        }
        if (MODE == 0) {
            const GAS u64* fl = (const GAS u64*)(ws + WS_FLAG) + cb * 32 + (lane & 31);
#pragma nounroll
            for (unsigned s = 0; s < SPIN_CAP; ++s) {
                const u64 v = __hip_atomic_load(fl, RLX_AGENT);
                if (__all(v == tag)) break;
                __builtin_amdgcn_s_sleep(1);
            }
            CBAR();
        }
        STAMP(0, 4);
        u32x4 xv[4];
        {
            const unsigned xo = WS_XT + ((unsigned)conv * D + c) * (L * 2) + lane * 16;
#pragma unroll
            for (int i = 0; i < 4; ++i) xv[i] = __builtin_amdgcn_raw_buffer_load_b128(rs, xo + i * 1024, 0, 16);
        }
        if (!conv) lds_wait_ge(sw + SW_KR + (wv >> 1), 2);
        STAMP(0, 5);

        const int g = lane >> 4, q = (lane >> 2) & 3, p = lane & 3;
        const char LDSP* xb = xs + 2 * (XPAD + 12 + 16 * p - 32 * g - 16 - 4 * q);
        const char LDSP* fb = fs + 2 * (FPAD - 12 + 4 * p + 32 * g + 4 * q);
        u16 LDSP* y4 = (u16 LDSP*)(smem + Y_OFF) + lane * 4 + wv;
        unsigned LDSP* prog = sw + SW_PROG + wv;

        s16x8 xf[32], ff[16];
        f32x4 acc[32];
        float c1 = 0.f, c2 = 0.f, c3 = 0.f, s1 = 0.f, s2 = 0.f, yv = 0.f;
        const bool last = (lane == 63);
        constexpr int PD = 3;
#define LOADFR(T)                                                                          \
    do {                                                                                   \
        if constexpr (((T) & 7) == 0) *(u32x4 LDSP*)(xs + XPAD * 2 + ((T) >> 3) * 1024 + lane * 16) = xv[(T) >> 3]; \
        xf[T] = frag(xb + 128 * (T) + 32, xb + 128 * (T));                                 \
        if constexpr (((T) & 1) == 0) ff[(T) >> 1] = frag(fb + 256 * ((T) >> 1), fb + 256 * ((T) >> 1) + 32); \
    } while (0)
#define FIN(T, S)                                                                          \
    do {                                                                                   \
        if constexpr ((S) == 0) { s2 = acc[T][2] + ror1(last ? c3 : acc[T][3]); c3 = acc[T][3]; }    \
        if constexpr ((S) == 1) { s1 = acc[T][1] + ror1(last ? c2 : s2); c2 = s2; }                  \
        if constexpr ((S) == 2) { yv = acc[T][0] + ror1(last ? c1 : s1); c1 = s1; }                  \
        if constexpr ((S) == 3) { y4[(T) * 256] = (u16)cvtpk(yv, yv); CBAR(); lds_st(prog, (T) + 1); } \
    } while (0)
        static_for<0, PD>([&](auto tc) { constexpr int t = decltype(tc)::value; LOADFR(t); });
        SBAR();
        static_for<0, 32>([&](auto phc) {
            constexpr int ph = decltype(phc)::value;
            if constexpr (ph + PD < 32) LOADFR(ph + PD);
            SBAR();
            constexpr int n = (ph >> 1) + 1;
            f32x4 ac = {0.f, 0.f, 0.f, 0.f};
            static_for<0, n>([&](auto kpc) {
                constexpr int kp = decltype(kpc)::value;
                ac = __builtin_amdgcn_mfma_f32_16x16x32_bf16(xf[ph - 2 * kp], ff[kp], ac, 0, 0, 0);
                if constexpr (ph >= 2) {
                    if constexpr (kp < 4) FIN(ph - 2, kp);
                    if constexpr (kp == n - 1) {
                        static_for<(n < 4 ? n : 4), 4>([&](auto sc) { constexpr int s = decltype(sc)::value; FIN(ph - 2, s); });
                    }
                }
                SBAR();
            });
            acc[ph] = ac;
        });
        FIN(30, 0); FIN(30, 1); FIN(30, 2); FIN(30, 3);
        FIN(31, 0); FIN(31, 1); FIN(31, 2); FIN(31, 3);
        STAMP(0, 6);
    } else {
        if (MODE == 1) { __builtin_amdgcn_s_barrier(); return; }
        const int kw = wv - 4;
        const int chs = kw >> 1;
        const int c = cbase + chs;
        const int n = (kw & 1) * 64 + lane;
        const int pi = c * NM + n;
        const u16 r_are = a.Are[pi], r_aim = a.Aim[pi], r_bre = a.Bre[pi], r_bim = a.Bim[pi], r_cre = a.Cre[pi], r_cim = a.Cim[pi];
        const u16 r_ldt = a.logdt[c];
        const unsigned r_ds = *(const unsigned*)(a.dskip + cbase), r_sd = *(const unsigned*)(a.ssmd + cbase);
        __builtin_amdgcn_s_barrier();
        CBAR();
        const float dt = rbf(expf(bf2f(r_ldt)));
        const float ar = dt * bf2f(r_are), ai = dt * bf2f(r_aim);
        const cf dB = {dt * bf2f(r_bre), dt * bf2f(r_bim)};
        const cf Cc = {bf2f(r_cre), bf2f(r_cim)};
        const cf CB = cmul(Cc, dB);
        const float lr = ar * 1.4426950408889634f;
        const float I2PH = 0.15915494309189535f;
        const float I2PL = (float)(0.15915494309189535 - (double)0.15915494309189535f);
        const float fh = ai * I2PH;
        const float fl = __builtin_fmaf(ai, I2PH, -fh) + ai * I2PL;
        STAMP(0, 1);

        char LDSP* by = smem + KG_OFF + kw * KG_WAVE;
        char LDSP* bxa = by + KG_BUF;
        char LDSP* bxb = by + 2 * KG_BUF;
        const int fr = lane & 15, fg = lane >> 4;
        {
            cf y[16];
            y[0] = cf{1.f, 0.f};
            y[1] = cexp_p2(lr, fh, fl, 8.f);
            y[2] = cexp_p2(lr, fh, fl, 16.f);
            y[4] = cexp_p2(lr, fh, fl, 32.f);
            y[8] = cexp_p2(lr, fh, fl, 64.f);
            y[3] = cmul(y[1], y[2]);
            y[5] = cmul(y[1], y[4]);
            y[6] = cmul(y[2], y[4]);
            y[7] = cmul(y[3], y[4]);
#pragma unroll
            for (int i = 1; i < 8; ++i) y[8 + i] = cmul(y[i], y[8]);
#pragma unroll
            for (int i = 0; i < 16; ++i) kg_put(by, i, lane, y[i], true);
        }
        cf e[8];
        e[1] = cexp_p2(lr, fh, fl, 1.f);
        e[2] = cexp_p2(lr, fh, fl, 2.f);
        e[4] = cexp_p2(lr, fh, fl, 4.f);
        e[3] = cmul(e[1], e[2]);
        e[5] = cmul(e[1], e[4]);
        e[6] = cmul(e[2], e[4]);
        e[7] = cmul(e[3], e[4]);
        cf G[8];
        const cf p1 = cexp_p2(lr, fh, fl, 128.f), p2 = cexp_p2(lr, fh, fl, 256.f), p4 = cexp_p2(lr, fh, fl, 512.f);
        const cf p8 = cexp_p2(lr, fh, fl, 1024.f), p16 = cexp_p2(lr, fh, fl, 2048.f);
        G[0] = CB;
        G[1] = cmul(G[0], p1);
        G[2] = cmul(G[0], p2);
        G[3] = cmul(G[1], p2);
#pragma unroll
        for (int i = 0; i < 4; ++i) G[4 + i] = cmul(G[i], p4);
#pragma unroll
        for (int gg = 0; gg < 2; ++gg) {
            kg_put(bxa, 8 * gg, lane, G[gg], false);
#pragma unroll
            for (int r = 1; r < 8; ++r) kg_put(bxa, 8 * gg + r, lane, cmul(G[gg], e[r]), false);
        }
        s16x8 yh[4], yl[4];
#pragma unroll
        for (int ks = 0; ks < 4; ++ks) {
            const int yoff = fr * KG_ROWB + (((ks * 4 + fg) ^ fr) << 4);
            yh[ks] = *(const s16x8 LDSP*)(by + yoff);
            yl[ks] = *(const s16x8 LDSP*)(by + 16 * KG_ROWB + yoff);
        }
        const f32x4 acc0 = kg_mfma(bxa, yh, yl, fr, fg, 16);
#pragma unroll
        for (int gg = 2; gg < 4; ++gg) {
            kg_put(bxb, 4 * (gg - 2), lane, G[gg], false);
#pragma unroll
            for (int r = 1; r < 4; ++r) kg_put(bxb, 4 * (gg - 2) + r, lane, cmul(G[gg], e[2 * r]), false);
        }
#pragma unroll
        for (int gg = 4; gg < 8; ++gg) {
            kg_put(bxb, 8 + 2 * (gg - 4), lane, G[gg], false);
            kg_put(bxb, 8 + 2 * (gg - 4) + 1, lane, cmul(G[gg], e[4]), false);
        }
        const f32x4 acc1 = kg_mfma(bxb, yh, yl, fr, fg, 16);
#pragma unroll
        for (int gg = 0; gg < 8; ++gg) kg_put(by, gg, lane, cmul(G[gg], p8), false);
        kg_put(by, 8, lane, cmul(G[0], p16), false);
        const f32x4 acc2 = kg_mfma(by, yh, yl, fr, fg, 9);
        STAMP(0, 2);
        float LDSP* kq = (float LDSP*)bxa;
#pragma unroll
        for (int j = 0; j < 4; ++j) {
            const int row = fg * 4 + j;
            kq[(row >> 3) * 128 + (row & 7) + 8 * fr] = acc0[j];
            const int l1 = (row < 8) ? (256 + (row >> 2) * 128 + (row & 3) * 2) : (512 + ((row - 8) >> 1) * 128 + ((row - 8) & 1) * 4);
            kq[l1 + 8 * fr] = acc1[j];
            const int l2 = 1024 + row * 128 + 8 * fr;
            if (l2 <= 2048) kq[l2] = acc2[j];
        }
        lds_arrive(sw + SW_KP + chs, lane);
        lds_wait_ge(sw + SW_KP + chs, 2);
        STAMP(0, 3);
        {
            const float LDSP* k0 = (const float LDSP*)(smem + KG_OFF + (2 * chs) * KG_WAVE + KG_BUF);
            const float LDSP* k1 = (const float LDSP*)(smem + KG_OFF + (2 * chs + 1) * KG_WAVE + KG_BUF);
            unsigned LDSP* fdst = (unsigned LDSP*)(smem + (2 * chs) * CC_B + XS_B + FPAD * 2);
#pragma unroll
            for (int i = 0; i < 8; ++i) {
                const int dwi = (i * 2 + (kw & 1)) * 64 + lane;
                const int lp = dwi * 2;
                const unsigned qq = cvtpk((float)lp, (float)(lp + 1));
                const int qa = (int)bf_lo(qq), qb = (int)bf_hi(qq);
                fdst[dwi] = cvtpk(k0[qa] + k1[qa], k0[qb] + k1[qb]);
            }
        }
        lds_arrive(sw + SW_KR + chs, lane);
        STAMP(0, 4);
        if (MODE == 0) {
            lds_wait_ge(sw + SW_TAGR, 1);
            const u64 tag = (u64)lds_ld(sw + SW_TAG) | ((u64)lds_ld(sw + SW_TAG + 1) << 32);
            const GAS u64* fl = (const GAS u64*)(ws + WS_FLAG) + lane;
#pragma nounroll
            for (unsigned s = 0; s < SPIN_CAP; ++s) {
                const u64 v0 = __hip_atomic_load(fl, RLX_AGENT), v1 = __hip_atomic_load(fl + 64, RLX_AGENT);
                const u64 v2 = __hip_atomic_load(fl + 128, RLX_AGENT), v3 = __hip_atomic_load(fl + 192, RLX_AGENT);
                if (__all(v0 == tag && v1 == tag && v2 == tag && v3 == tag)) break;
                __builtin_amdgcn_s_sleep(2);
            }
            CBAR();
            if (blk == 0 && kw == 0 && lane == 0) __hip_atomic_store((GAS u64*)(ws + WS_GEN), tag, RLX_AGENT);
        }
        STAMP(0, 5);
        float gts[8];
#pragma unroll
        for (int i = 0; i < 8; ++i)
            gts[i] = __uint_as_float(__builtin_amdgcn_raw_buffer_load_b32(rs, WS_G + ((i * 4 + kw) * 64 + lane) * 4, 0, 16));
        const float ds0 = bf_lo(r_ds), ds1 = bf_hi(r_ds), sd0 = bf_lo(r_sd), sd1 = bf_hi(r_sd);
        const u16 LDSP* xu0 = (const u16 LDSP*)(smem + 0 * CC_B) + XPAD + kw * 64 + lane;
        const u16 LDSP* xk0 = (const u16 LDSP*)(smem + 1 * CC_B) + XPAD + kw * 64 + lane;
        const u16 LDSP* xu1 = (const u16 LDSP*)(smem + 2 * CC_B) + XPAD + kw * 64 + lane;
        const u16 LDSP* xk1 = (const u16 LDSP*)(smem + 3 * CC_B) + XPAD + kw * 64 + lane;
        const u32x2 LDSP* y4 = (const u32x2 LDSP*)(smem + Y_OFF) + kw * 64 + lane;
        unsigned* op = (unsigned*)(a.out + (size_t)(kw * 64 + lane) * D + cbase);
#pragma unroll
        for (int i = 0; i < 8; ++i) {
            const unsigned need = i * 4 + kw + 1;
#pragma nounroll
            for (unsigned s = 0; s < SPIN_CAP; ++s) {
                const unsigned p0 = lds_ld(sw + 0), p1 = lds_ld(sw + 1), p2 = lds_ld(sw + 2), p3 = lds_ld(sw + 3);
                const unsigned m = min(min(p0, p1), min(p2, p3));
                if ((unsigned)__builtin_amdgcn_readfirstlane(m) >= need) break;
                __builtin_amdgcn_s_sleep(1);
            }
            CBAR();
            const u32x2 yy = y4[i * 256];
            const float ua = bf2f(xu0[i * 256]), ub = bf2f(xu1[i * 256]), ka = bf2f(xk0[i * 256]), kb = bf2f(xk1[i * 256]);
            const float gt = gts[i];
            const float omg = rbf(1.0f - gt);
            unsigned pp = cvtpk(ua * ds0, ub * ds1);
            const unsigned av = cvtpk(bf_lo(yy[0]) + bf_lo(pp), bf_lo(yy[1]) + bf_hi(pp));
            pp = cvtpk(ka * sd0, kb * sd1);
            const unsigned yb = cvtpk(bf_hi(yy[0]) + bf_lo(pp), bf_hi(yy[1]) + bf_hi(pp));
            const unsigned bv = cvtpk(ua * bf_lo(yb), ub * bf_hi(yb));
            const unsigned ga = cvtpk(gt * bf_lo(av), gt * bf_hi(av));
            const unsigned gb = cvtpk(omg * bf_lo(bv), omg * bf_hi(bv));
            op[(size_t)i * 256 * (D / 2)] = cvtpk(bf_lo(ga) + bf_lo(gb), bf_hi(ga) + bf_hi(gb));
        }
        STAMP(0, 6);
    }
}

extern "C" void kernel_launch(void* const* d_in, const int* in_sizes, int n_in, void* d_out, int out_size, void* d_ws,
                              size_t ws_size, hipStream_t stream) {
    Args a;
    a.u = (const u16*)d_in[0];
    a.Are = (const u16*)d_in[1];
    a.Aim = (const u16*)d_in[2];
    a.Bre = (const u16*)d_in[3];
    a.Bim = (const u16*)d_in[4];
    a.Cre = (const u16*)d_in[5];
    a.Cim = (const u16*)d_in[6];
    a.logdt = (const u16*)d_in[7];
    a.dskip = (const u16*)d_in[8];
    a.kk = (const u16*)d_in[10];
    a.vv = (const u16*)d_in[11];
    a.shk = (const u16*)d_in[12];
    a.ssmk = (const u16*)d_in[13];
    a.ssmd = (const u16*)d_in[14];
    a.gw = (const u16*)d_in[15];
    a.out = (u16*)d_out;
    a.ws = (char*)d_ws;
    (void)hipFuncSetAttribute((const void*)fused_kernel<0>, hipFuncAttributeMaxDynamicSharedMemorySize, LDS_BYTES);
    fused_kernel<0><<<256, 512, LDS_BYTES, stream>>>(a);
}
```

```cpp
#include <hip/hip_runtime.h>
#include <hip/hip_bf16.h>
#include <stdint.h>
#define STAMP(k, i) do {} while (0)
#define STAMPC(k, i) do {} while (0)
#define PRIO_M 0
#define POLL_SLEEP ""
#define ROLE_SWAP 256
#define EXACT_KS 1

typedef unsigned short u16;
typedef unsigned long long u64;
typedef short s16x4 __attribute__((ext_vector_type(4)));
typedef short s16x8 __attribute__((ext_vector_type(8)));
typedef _Float16 f16x8 __attribute__((ext_vector_type(8)));
typedef float f32x4 __attribute__((ext_vector_type(4)));
typedef float f32x2 __attribute__((ext_vector_type(2)));
typedef unsigned u32x4 __attribute__((ext_vector_type(4)));
typedef unsigned u32x2 __attribute__((ext_vector_type(2)));
#define LDSP __attribute__((address_space(3)))
#define GAS __attribute__((address_space(1)))
#define RLX_AGENT __ATOMIC_RELAXED, __HIP_MEMORY_SCOPE_AGENT
#define RLX_WG __ATOMIC_RELAXED, __HIP_MEMORY_SCOPE_WORKGROUP
#define SBAR() __builtin_amdgcn_sched_barrier(0)
#define CBAR() asm volatile("" ::: "memory")
#define PIN(x) asm volatile("" : "+v"(x))

constexpr int L = 2048, D = 512, NM = 128;

constexpr unsigned WS_XT = 0;
constexpr unsigned WS_G = WS_XT + 2u * D * L * 2;
constexpr unsigned WS_FL = WS_G + L * 8;
constexpr unsigned WS_GEN = WS_FL + 256 * 128;
constexpr unsigned WS_USED = WS_GEN + 256;
constexpr u64 TAG_INC = 0x9E3779B97F4A7C15ull;

constexpr int XPAD = 128, FPAD = 16;
constexpr int XS_B = (XPAD + L) * 2;
constexpr int FS_B = (FPAD + L) * 2;
constexpr int CC_B = XS_B + FS_B;
constexpr int YF_OFF = 4 * CC_B;
constexpr int YF_B = L * 4 + 16;
constexpr int TS_OFF = YF_OFF + 4 * YF_B;
constexpr int SYNC_OFF = TS_OFF + 16384;
constexpr int KG_OFF = SYNC_OFF + 128;
constexpr int KG_ROWB = 528;
constexpr int KG_TAB = 57 * KG_ROWB;
constexpr int KG_Q = 4224;
constexpr int KG_CH = KG_TAB + KG_Q;
constexpr int LDS_BYTES = KG_OFF + 2 * KG_CH;
enum { SW_PROG = 0, SW_T1 = 4, SW_T2 = 5, SW_KT = 6, SW_FR = 8, SW_G2 = 12, SW_GO = 13, SW_SC = 16 };

__device__ __forceinline__ float bf_lo(unsigned x) { return __uint_as_float(x << 16); }
__device__ __forceinline__ float bf_hi(unsigned x) { return __uint_as_float(x & 0xffff0000u); }
__device__ __forceinline__ float bf2f(u16 x) { return __uint_as_float(((unsigned)x) << 16); }
__device__ __forceinline__ f32x2 bf_pair(unsigned x) { return f32x2{bf_lo(x), bf_hi(x)}; }
__device__ __forceinline__ unsigned cvtpk(float lo, float hi) {
    typedef __bf16 bf16x2 __attribute__((ext_vector_type(2)));
    f32x2 v = {lo, hi};
    bf16x2 r = __builtin_convertvector(v, bf16x2);
    return __builtin_bit_cast(unsigned, r);
}
__device__ __forceinline__ unsigned cvtpk_h(f32x2 v) {
    typedef _Float16 f16x2 __attribute__((ext_vector_type(2)));
    f16x2 r = __builtin_convertvector(v, f16x2);
    return __builtin_bit_cast(unsigned, r);
}
__device__ __forceinline__ float rbf(float x) { return bf_lo(cvtpk(x, x)); }
__device__ __forceinline__ f32x2 rnd2(f32x2 v) { return bf_pair(cvtpk(v[0], v[1])); }
__device__ __forceinline__ float exp_bf(float x) {
    const float LH = 1.44268798828125f;
    const float LL = (float)((1.4426950408889634 - 1.44268798828125) * 0.6931471805599453);
    const float r = __builtin_amdgcn_exp2f(x * LH);
    const float c = x * LL;
    return __builtin_fmaf(r, __builtin_fmaf(0.5f * c, c, c), r);
}
__device__ __forceinline__ float epi1(float ya, float yb, float u, float kv, float g, float omg, float ds, float sd) {
    float a, b;
    asm("v_fma_f32 %0, %4, %8, %2\n\t"
        "v_fma_f32 %1, %5, %9, %3\n\t"
        "v_mul_f32 %0, %6, %0\n\t"
        "v_mul_f32 %1, %4, %1\n\t"
        "v_fma_f32 %0, %7, %1, %0"
        : "=&v"(a), "=&v"(b) : "v"(ya), "v"(yb), "v"(u), "v"(kv), "v"(g), "v"(omg), "v"(ds), "v"(sd));
    return a;
}

__device__ __forceinline__ void lds_wait_ge(unsigned addr, unsigned need) {
    unsigned v, s, cnt;
    asm volatile("s_mov_b32 %2, 0x400000\n"
                 "1:\n\t"
                 "ds_read_b32 %0, %3\n\t"
                 "s_waitcnt lgkmcnt(0)\n\t"
                 "v_readfirstlane_b32 %1, %0\n\t"
                 "s_sub_u32 %2, %2, 1\n\t"
                 "s_cselect_b32 %1, -1, %1\n\t"
                 "s_cmp_ge_u32 %1, %4\n\t"
                 "s_cbranch_scc0 1b"
                 : "=&v"(v), "=&s"(s), "=&s"(cnt) : "v"(addr), "s"(need) : "memory", "scc");
}
__device__ __forceinline__ void lds_wait_all_ge(unsigned addr, unsigned need) {
    unsigned v, cnt;
    asm volatile("s_mov_b32 %1, 0x400000\n"
                 "1:\n\t"
                 POLL_SLEEP
                 "ds_read_b32 %0, %2\n\t"
                 "s_waitcnt lgkmcnt(0)\n\t"
                 "v_cmp_gt_u32 vcc, %3, %0\n\t"
                 "s_sub_u32 %1, %1, 1\n\t"
                 "s_cselect_b64 vcc, 0, vcc\n\t"
                 "s_cbranch_vccnz 1b"
                 : "=&v"(v), "=&s"(cnt) : "v"(addr), "s"(need) : "memory", "scc", "vcc");
}
__device__ __forceinline__ void lds_arrive(unsigned addr) {
    u64 sv; unsigned one = 1;
    asm volatile("s_mov_b64 %0, exec\n\t"
                 "s_mov_b64 exec, 1\n\t"
                 "ds_add_u32 %1, %2\n\t"
                 "s_mov_b64 exec, %0"
                 : "=&s"(sv) : "v"(addr), "v"(one) : "memory");
}
__device__ __forceinline__ unsigned lds_arrive_rtn(unsigned addr) {
    u64 sv; unsigned one = 1, r, s;
    asm volatile("s_mov_b64 %0, exec\n\t"
                 "s_mov_b64 exec, 1\n\t"
                 "ds_add_rtn_u32 %1, %3, %4\n\t"
                 "s_waitcnt lgkmcnt(0)\n\t"
                 "v_readfirstlane_b32 %2, %1\n\t"
                 "s_mov_b64 exec, %0"
                 : "=&s"(sv), "=&v"(r), "=&s"(s) : "v"(addr), "v"(one) : "memory");
    return s;
}

template <int CTRL, int RMASK>
__device__ __forceinline__ float dpp_add(float x) {
    return x + __int_as_float(__builtin_amdgcn_update_dpp(0, __float_as_int(x), CTRL, RMASK, 0xf, true));
}
__device__ __forceinline__ float wave_sum63(float x) {
    x = dpp_add<0x111, 0xf>(x);
    x = dpp_add<0x112, 0xf>(x);
    x = dpp_add<0x114, 0xf>(x);
    x = dpp_add<0x118, 0xf>(x);
    x = dpp_add<0x142, 0xa>(x);
    x = dpp_add<0x143, 0xc>(x);
    return x;
}
template <int CTRL, int RMASK>
__device__ __forceinline__ unsigned dpp_maxu(unsigned x) {
    return max(x, (unsigned)__builtin_amdgcn_update_dpp(0, (int)x, CTRL, RMASK, 0xf, true));
}
__device__ __forceinline__ unsigned wave_maxu(unsigned x) {
    x = dpp_maxu<0x111, 0xf>(x);
    x = dpp_maxu<0x112, 0xf>(x);
    x = dpp_maxu<0x114, 0xf>(x);
    x = dpp_maxu<0x118, 0xf>(x);
    x = dpp_maxu<0x142, 0xa>(x);
    x = dpp_maxu<0x143, 0xc>(x);
    return (unsigned)__builtin_amdgcn_readlane((int)x, 63);
}

typedef f32x2 cf;
__device__ __forceinline__ cf cmul(cf a, cf b) {
    cf r;
    asm("v_pk_mul_f32 %0, %1, %2 op_sel_hi:[0,1]\n\t"
        "v_pk_fma_f32 %0, %1, %2, %0 op_sel:[1,1,0] op_sel_hi:[1,0,1] neg_lo:[0,1,0]"
        : "=&v"(r) : "v"(a), "v"(b));
    return r;
}
__device__ __forceinline__ cf cexp_p2(float lr, float fh, float fl, float l) {
    const float mag = __builtin_amdgcn_exp2f(lr * l);
    const float ph = __builtin_amdgcn_fractf(fh * l) + fl * l;
    return cf{mag * __builtin_amdgcn_cosf(ph), mag * __builtin_amdgcn_sinf(ph)};
}
__device__ __forceinline__ s16x4 trread(const char LDSP* p) {
    return __builtin_amdgcn_ds_read_tr16_b64_v4i16((s16x4 LDSP*)p);
}

template <int V> struct ic { static constexpr int value = V; };
template <int B, int E, class F>
__device__ __forceinline__ void static_for(F&& f) {
    if constexpr (B < E) { f(ic<B>{}); static_for<B + 1, E>(f); }
}

constexpr int tile_start(int T) { int s = 0; for (int i = 0; i < T; ++i) s += (i >> 1) + 1; return s; }
constexpr int NMFMA = tile_start(32);
struct Sched { short xr[NMFMA + 64]; short fin[NMFMA + 64]; int nslot; };
constexpr Sched make_sched() {
    Sched S{};
    for (int i = 0; i < NMFMA + 64; ++i) { S.xr[i] = -1; S.fin[i] = -1; }
    for (int T = 8; T < 32; ++T) { S.xr[tile_start(T - 2)] = (short)(2 * T); S.xr[tile_start(T - 2) + 1] = (short)(2 * T + 1); }
    int g = 0;
    for (int T = 0; T < 32; ++T) {
        int first = tile_start(T + 1) - 1 + 4;
        if (first > NMFMA) first = NMFMA;
        if (g < first) g = first;
        for (int w = 0; w < ((T & 1) ? 8 : 7); ++w) S.fin[g++] = (short)(T * 8 + w);
    }
    S.nslot = g;
    return S;
}
constexpr Sched SCHED = make_sched();

struct Args {
    const u16 *u, *kk, *vv, *shk, *gw, *Are, *Aim, *Bre, *Bim, *Cre, *Cim, *logdt, *dskip, *ssmk, *ssmd;
    u16* out;
    char* ws;
};

__global__ __launch_bounds__(512, 2) void fused_kernel(Args a) {
    extern __shared__ __attribute__((aligned(16))) char smem_g[];
    char LDSP* smem = (char LDSP*)smem_g;
    char* ws = a.ws;
    const int tid = threadIdx.x ^ ROLE_SWAP, lane = tid & 63;
    const int wv = __builtin_amdgcn_readfirstlane(tid >> 6);
    const int blk = blockIdx.x;
    const int cb = blk & 7, tb = blk >> 3;
    const int cbase = cb * 64 + tb * 2;
    const unsigned swb = (unsigned)(uintptr_t)(smem + SYNC_OFF);
#define SWA(i) (swb + 4u * (unsigned)(i))
    const __amdgpu_buffer_rsrc_t rs = __builtin_amdgcn_make_buffer_rsrc(ws, 0, WS_USED, 0x00020000);
    STAMP(0, 0);

    if (__builtin_expect(wv < 4, 1)) {
        __builtin_amdgcn_s_setprio(2);
        const int cg = tid & 7, rg = tid >> 3;
        const int c0 = cb * 64 + cg * 8;
        const int t0 = tb * 64 + rg * 2;
        u32x4 ur[2], vr[2], kr[5], sk[4];
#pragma unroll
        for (int i = 0; i < 5; ++i) {
            const int t = t0 - 3 + i;
            kr[i] = *(const u32x4*)(a.kk + (size_t)(t < 0 ? 0 : t) * D + c0);
        }
#pragma unroll
        for (int i = 0; i < 4; ++i) sk[i] = *(const u32x4*)(a.shk + (size_t)c0 * 4 + i * 8);
#pragma unroll
        for (int i = 0; i < 2; ++i) vr[i] = *(const u32x4*)(a.vv + (size_t)(t0 + i) * D + c0);
#pragma unroll
        for (int i = 0; i < 2; ++i) ur[i] = *(const u32x4*)(a.u + (size_t)(t0 + i) * D + c0);
        SBAR();
        const int gr = blk * 8 + wv * 2;
        u32x4 gx[2];
#pragma unroll
        for (int i = 0; i < 2; ++i) gx[i] = *(const u32x4*)(a.u + (size_t)(gr + i) * D + lane * 8);
        const u32x4 gwv = *(const u32x4*)(a.gw + lane * 8);
        const int conv = wv & 1, c = cbase + (wv >> 1);
        char LDSP* xs = smem + wv * CC_B;
        char LDSP* fs = xs + XS_B;
        {
            const u32x4 z = {0u, 0u, 0u, 0u};
            char LDSP* zp = lane < 16 ? xs + lane * 16 : (lane < 18 ? fs + (lane - 16) * 16 : smem + SYNC_OFF + (lane & 7) * 16);
            *(u32x4 LDSP*)zp = z;
        }
        u64 tag;
        SBAR();
        asm volatile("s_load_dwordx2 %0, %1, 0x0\n\ts_waitcnt lgkmcnt(0)" : "=s"(tag) : "s"(ws + WS_GEN) : "memory");
        tag += TAG_INC;
        __builtin_amdgcn_s_barrier();
        CBAR();
        SBAR();

        unsigned LDSP* lu = (unsigned LDSP*)(smem + TS_OFF);
        unsigned LDSP* lkv = lu + 64 * 32;
        if (__builtin_expect(tb == 0, 0)) {
#pragma unroll
            for (int i = 0; i < 3; ++i)
                if (t0 - 3 + i < 0) kr[i] = u32x4{0u, 0u, 0u, 0u};
        }
        const int col = cg * 8 * 32 + (rg ^ (cg << 2));
#pragma unroll
        for (int h = 0; h < 4; ++h) {
            const f32x2 W0 = {bf_lo(sk[h][0]), bf_lo(sk[h][2])}, W1 = {bf_hi(sk[h][0]), bf_hi(sk[h][2])};
            const f32x2 W2 = {bf_lo(sk[h][1]), bf_lo(sk[h][3])}, W3 = {bf_hi(sk[h][1]), bf_hi(sk[h][3])};
            f32x2 K[5];
#pragma unroll
            for (int i = 0; i < 5; ++i) K[i] = bf_pair(kr[i][h]);
            f32x2 sa = rnd2(K[0] * W3), sb = rnd2(K[1] * W3);
            sa = rnd2(sa + rnd2(K[1] * W2)); sb = rnd2(sb + rnd2(K[2] * W2));
            sa = rnd2(sa + rnd2(K[2] * W1)); sb = rnd2(sb + rnd2(K[3] * W1));
            sa = rnd2(sa + rnd2(K[3] * W0)); sb = rnd2(sb + rnd2(K[4] * W0));
            const f32x2 ka = sa * bf_pair(vr[0][h]);
            const f32x2 kb = sb * bf_pair(vr[1][h]);
            lkv[col + (2 * h) * 32] = cvtpk(ka[0], kb[0]);
            lkv[col + (2 * h + 1) * 32] = cvtpk(ka[1], kb[1]);
            lu[col + (2 * h) * 32] = __builtin_amdgcn_perm(ur[1][h], ur[0][h], 0x05040100u);
            lu[col + (2 * h + 1) * 32] = __builtin_amdgcn_perm(ur[1][h], ur[0][h], 0x07060302u);
        }
        STAMP(0, 1);
        lds_arrive(SWA(SW_T1));
        lds_wait_ge(SWA(SW_T1), 4);
        {
            const int sg = tid & 7, chl = tid >> 3;
#pragma unroll
            for (int h = 0; h < 2; ++h) {
                const int ch = chl + 32 * h;
                const int off = ch * 32 + ((sg ^ ((ch >> 3) & 7)) << 2);
                const u32x4 va = *(const u32x4 LDSP*)(lu + off);
                const u32x4 vb = *(const u32x4 LDSP*)(lkv + off);
                const unsigned go = ((unsigned)(cb * 64 + ch) * L + tb * 64 + sg * 8) * 2;
                __builtin_amdgcn_raw_buffer_store_b128(va, rs, WS_XT + go, 0, 16);
                __builtin_amdgcn_raw_buffer_store_b128(vb, rs, WS_XT + D * L * 2 + go, 0, 16);
            }
        }
        STAMP(0, 2);
        SBAR();
        {
            float s[2];
#pragma unroll
            for (int i = 0; i < 2; ++i) {
                f32x2 acc = {0.f, 0.f};
#pragma unroll
                for (int d = 0; d < 4; ++d) acc += rnd2(bf_pair(gx[i][d]) * bf_pair(gwv[d]));
                s[i] = wave_sum63(acc[0] + acc[1]);
            }
            const unsigned sp = cvtpk(s[0], s[1]);
            const float e0 = __builtin_amdgcn_exp2f(-1.4426950408889634f * bf_lo(sp));
            const float e1 = __builtin_amdgcn_exp2f(-1.4426950408889634f * bf_hi(sp));
            const unsigned gp = cvtpk(__builtin_amdgcn_rcpf(1.0f + e0), __builtin_amdgcn_rcpf(1.0f + e1));
            const unsigned og = cvtpk(1.0f - bf_lo(gp), 1.0f - bf_hi(gp));
            if (lane == 63) {
                GAS u64* gd = (GAS u64*)(ws + WS_G) + gr;
                __hip_atomic_store(gd, (u64)((gp & 0xffffu) | (og << 16)) | ((u64)(unsigned)tag << 32), RLX_AGENT);
                __hip_atomic_store(gd + 1, (u64)((gp >> 16) | (og & 0xffff0000u)) | ((u64)(unsigned)tag << 32), RLX_AGENT);
            }
        }
        SBAR();
        asm volatile("s_waitcnt vmcnt(2)" ::: "memory");
        STAMP(0, 3);
        if (lds_arrive_rtn(SWA(SW_T2)) == 3) {
            if (lane == 0) __hip_atomic_store((GAS u64*)(ws + WS_FL + (cb * 32 + tb) * 128), tag, RLX_AGENT);
        }
        const int g = lane >> 4, q = (lane >> 2) & 3, p = lane & 3;
        const char LDSP* xb = xs + 2 * (XPAD + 12 + 16 * p - 32 * g - 16 - 4 * q);
        const char LDSP* fb = fs + 2 * (FPAD - 12 + 4 * p + 32 * g + 4 * q);
        float LDSP* yf = (float LDSP*)(smem + YF_OFF + wv * YF_B) + lane;
        unsigned LDSP* prog = (unsigned LDSP*)(smem + SYNC_OFF) + SW_PROG + wv;
        const unsigned xo = WS_XT + ((unsigned)conv * D + c) * (L * 2) + lane * 16;
        {
            const unsigned fo = WS_FL + (cb * 32 + (lane & 31)) * 128;
            u64 v; unsigned cnt;
            asm volatile("s_mov_b32 %1, 0x100000\n"
                         "1:\n\t"
                         "global_load_dwordx2 %0, %2, %3 sc1\n\t"
                         "s_waitcnt vmcnt(0)\n\t"
                         "v_cmp_ne_u64 vcc, %4, %0\n\t"
                         "s_sub_u32 %1, %1, 1\n\t"
                         "s_cselect_b64 vcc, 0, vcc\n\t"
                         "s_cbranch_vccnz 1b"
                         : "=&v"(v), "=&s"(cnt) : "v"(fo), "s"(ws), "s"(tag) : "memory", "scc", "vcc");
        }
        STAMP(0, 4);
        u32x4 xv[4];
#pragma unroll
        for (int i = 0; i < 4; ++i) xv[i] = __builtin_amdgcn_raw_buffer_load_b128(rs, xo + i * 1024, 0, 16);
        ((unsigned LDSP*)(smem + SYNC_OFF))[SW_GO] = 1u;
        __builtin_amdgcn_s_setprio(PRIO_M);
        lds_wait_ge(SWA(SW_FR + wv), 2);

        s16x4 xh[64], fh_[32];
        f32x4 acc[32];
        static_for<0, 32>([&](auto ic_) { constexpr int i = decltype(ic_)::value; fh_[i] = trread(fb + 256 * (i >> 1) + 32 * (i & 1)); });
        STAMP(0, 5);
#define XFRAG(T) __builtin_shufflevector(xh[2 * (T)], xh[2 * (T) + 1], 0, 1, 2, 3, 4, 5, 6, 7)
#define FFRAG(K) __builtin_shufflevector(fh_[2 * (K)], fh_[2 * (K) + 1], 0, 1, 2, 3, 4, 5, 6, 7)
        float c1 = 0.f, c2 = 0.f, c3 = 0.f, s1 = 0.f, s2 = 0.f, tm = 0.f, yv = 0.f;
        unsigned lastm = lane == 63 ? 0xffffffffu : 0u;
        PIN(lastm);
#define XREAD(H)                                                                                          \
    do {                                                                                                  \
        if constexpr (((H) & 15) == 0) *(u32x4 LDSP*)(xs + XPAD * 2 + ((H) >> 4) * 1024 + lane * 16) = xv[(H) >> 4]; \
        xh[H] = trread(xb + 128 * ((H) >> 1) + (((H) & 1) ? 0 : 32));                                     \
    } while (0)
#define BFI(d, c, v) asm volatile("v_bfi_b32 %0, %1, %2, %3" : "=v"(d) : "v"(lastm), "v"(c), "v"(v))
#define ADDROR(d, t, v, NP)                                                                               \
    do {                                                                                                  \
        if constexpr ((NP) == 0) asm volatile("v_add_f32_dpp %0, %1, %2 wave_ror:1 row_mask:0xf bank_mask:0xf" : "=v"(d) : "v"(t), "v"(v)); \
        if constexpr ((NP) == 1) asm volatile("s_nop 0\n\tv_add_f32_dpp %0, %1, %2 wave_ror:1 row_mask:0xf bank_mask:0xf" : "=v"(d) : "v"(t), "v"(v)); \
        if constexpr ((NP) == 2) asm volatile("s_nop 1\n\tv_add_f32_dpp %0, %1, %2 wave_ror:1 row_mask:0xf bank_mask:0xf" : "=v"(d) : "v"(t), "v"(v)); \
    } while (0)
#define FIN(T, W, NP)                                                                                     \
    do {                                                                                                  \
        if constexpr ((W) == 0) { BFI(tm, c3, acc[T][3]); c3 = acc[T][3]; }                               \
        if constexpr ((W) == 1) ADDROR(s2, tm, acc[T][2], NP);                                            \
        if constexpr ((W) == 2) { BFI(tm, c2, s2); c2 = s2; }                                             \
        if constexpr ((W) == 3) ADDROR(s1, tm, acc[T][1], NP);                                            \
        if constexpr ((W) == 4) { BFI(tm, c1, s1); c1 = s1; }                                             \
        if constexpr ((W) == 5) ADDROR(yv, tm, acc[T][0], NP);                                            \
        if constexpr ((W) == 6) yf[(T) * 64] = yv;                                                        \
        if constexpr ((W) == 7) { CBAR(); __hip_atomic_store(prog, (unsigned)((T) + 1), RLX_WG); }        \
    } while (0)
        static_for<0, 16>([&](auto hc) { constexpr int h = decltype(hc)::value; XREAD(h); });
        SBAR();
        STAMP(0, 7); STAMPC(1, 0);
        static_for<0, 32>([&](auto phc) {
            constexpr int ph = decltype(phc)::value;
            constexpr int n = (ph >> 1) + 1;
            f32x4 ac;
            static_for<0, n>([&](auto kpc) {
                constexpr int kp = decltype(kpc)::value;
                constexpr int k2 = n - 1 - kp;
                constexpr int m = tile_start(ph) + kp;
                const s16x8 av = XFRAG(ph - 2 * k2), bv = FFRAG(k2);
                if constexpr (kp == 0) asm volatile("v_mfma_f32_16x16x32_bf16 %0, %1, %2, 0" : "=&v"(ac) : "v"(av), "v"(bv));
                else asm volatile("v_mfma_f32_16x16x32_bf16 %0, %1, %2, %0" : "+v"(ac) : "v"(av), "v"(bv));
                if constexpr (kp == n - 1) acc[ph] = ac;
                SBAR();
                if constexpr (SCHED.xr[m] >= 0) { XREAD(SCHED.xr[m]); SBAR(); }
                if constexpr (SCHED.fin[m] >= 0) FIN(SCHED.fin[m] >> 3, SCHED.fin[m] & 7, (SCHED.xr[m] >= 0 ? 0 : 1));
                SBAR();
            });
        });
        asm volatile("s_nop 7\n\ts_nop 1" ::: "memory");
        static_for<NMFMA, SCHED.nslot>([&](auto mc) { constexpr int m = decltype(mc)::value; FIN(SCHED.fin[m] >> 3, SCHED.fin[m] & 7, 2); SBAR(); });
        STAMPC(1, 1); STAMP(0, 6);
    } else {
        const int kw = wv - 4;
        const int chs = kw >> 1, mh = kw & 1;
        const int c = cbase + chs;
        const int n = mh * 64 + lane;
        const int pi = c * NM + n;
        const u16 r_are = a.Are[pi], r_aim = a.Aim[pi], r_bre = a.Bre[pi], r_bim = a.Bim[pi], r_cre = a.Cre[pi], r_cim = a.Cim[pi];
        const u16 r_ldt = a.logdt[c];
        const unsigned r_ds = *(const unsigned*)(a.dskip + cbase), r_sd = *(const unsigned*)(a.ssmd + cbase);
        u64 tag;
        SBAR();
        asm volatile("s_load_dwordx2 %0, %1, 0x0\n\ts_waitcnt lgkmcnt(0)" : "=s"(tag) : "s"(ws + WS_GEN) : "memory");
        tag += TAG_INC;
        __builtin_amdgcn_s_barrier();
        CBAR();
        SBAR();
        const float dt = rbf(exp_bf(bf2f(r_ldt)));
        const float ar = dt * bf2f(r_are), ai = dt * bf2f(r_aim);
        cf CB = cmul(cf{bf2f(r_cre), bf2f(r_cim)}, cf{dt * bf2f(r_bre), dt * bf2f(r_bim)});
        const float lr = ar * 1.4426950408889634f;
        const float I2PH = 0.15915494309189535f;
        const float I2PL = (float)(0.15915494309189535 - (double)0.15915494309189535f);
        const float fh = ai * I2PH;
        const float fl = __builtin_fmaf(ai, I2PH, -fh) + ai * I2PL;
        int ex, ey;
        {
            const unsigned mcb = wave_maxu(max(__float_as_uint(CB[0]) & 0x7fffffffu, __float_as_uint(CB[1]) & 0x7fffffffu));
            int ecb = (int)(mcb >> 23) - 127;
            ecb = ecb < -96 ? -96 : ecb;
            int gx = 0, gy = 0;
            if (__builtin_expect(__any(lr > 0.f), 0)) {
                const float grow = __uint_as_float(wave_maxu(__float_as_uint(fmaxf(lr, 0.f))));
                gx = (int)fminf(ceilf(grow * 2048.f), 120.f);
                gy = (int)fminf(ceilf(grow * 120.f), 60.f);
            }
            ex = 13 - ecb - gx;
            ex = ex < -80 ? -80 : ex;
            ey = 14 - gy;
        }
        CB *= __uint_as_float((unsigned)(ex + 127) << 23);
        STAMP(0, 1);

        char LDSP* tabc = smem + KG_OFF + chs * KG_CH;
        unsigned LDSP* tp = (unsigned LDSP*)(tabc + n * 4);
#define PUT(row, v) __hip_atomic_store(tp + (row) * (KG_ROWB / 4), cvtpk_h(v), RLX_WG)
        ((float LDSP*)(smem + SYNC_OFF))[SW_SC + kw] = __uint_as_float((unsigned)(127 - ex - ey) << 23);
        {
            const float sy = __uint_as_float((unsigned)(ey + 127) << 23);
            const cf d0 = cexp_p2(lr, -fh, -fl, 8.f), d1 = cmul(d0, d0), d2 = cmul(d1, d1), d3 = cmul(d2, d2);
            cf y[8];
            y[0] = cf{sy, 0.f};
            y[1] = d0 * sy;
            y[2] = d1 * sy;
            y[4] = d2 * sy;
            y[3] = cmul(y[1], d1);
            y[5] = cmul(y[1], d2);
            y[6] = cmul(y[2], d2);
            y[7] = cmul(y[3], d2);
#pragma unroll
            for (int i = 0; i < 8; ++i) PUT(41 + i, y[i]);
            PUT(49, d3 * sy);
#pragma unroll
            for (int i = 1; i < 8; ++i) PUT(49 + i, cmul(y[i], d3));
        }
        {
            const cf p1 = cexp_p2(lr, fh, fl, 128.f), p2 = cmul(p1, p1), p4 = cmul(p2, p2), p8 = cmul(p4, p4), p16 = cmul(p8, p8);
            cf G[8];
            G[0] = CB;
            G[1] = cmul(G[0], p1);
            G[2] = cmul(G[0], p2);
            G[3] = cmul(G[1], p2);
#pragma unroll
            for (int i = 0; i < 4; ++i) G[4 + i] = cmul(G[i], p4);
#pragma unroll
            for (int gg = 0; gg < 8; ++gg) PUT(32 + gg, cmul(G[gg], p8));
            PUT(40, cmul(G[0], p16));
            cf e[8];
            e[1] = cexp_p2(lr, fh, fl, 1.f);
            e[2] = cmul(e[1], e[1]);
            e[4] = cmul(e[2], e[2]);
            e[3] = cmul(e[1], e[2]);
            e[5] = cmul(e[1], e[4]);
            e[6] = cmul(e[2], e[4]);
            e[7] = cmul(e[3], e[4]);
#pragma unroll
            for (int gg = 0; gg < 2; ++gg) {
                PUT(8 * gg, G[gg]);
#pragma unroll
                for (int r = 1; r < 8; ++r) PUT(8 * gg + r, cmul(G[gg], e[r]));
            }
#pragma unroll
            for (int gg = 2; gg < 4; ++gg) {
                PUT(16 + 4 * (gg - 2), G[gg]);
#pragma unroll
                for (int r = 1; r < 4; ++r) PUT(16 + 4 * (gg - 2) + r, cmul(G[gg], e[2 * r]));
            }
#pragma unroll
            for (int gg = 4; gg < 8; ++gg) {
                PUT(24 + 2 * (gg - 4), G[gg]);
                PUT(24 + 2 * (gg - 4) + 1, cmul(G[gg], e[4]));
            }
        }
        lds_arrive(SWA(SW_KT + chs));
        STAMP(0, 2);
        u32x4 fv[2];
#pragma unroll
        for (int i = 0; i < 2; ++i) fv[i] = *(const u32x4*)(a.ssmk + (size_t)c * L + (mh * 2 + i) * 512 + lane * 8);
        u16 LDSP* kq = (u16 LDSP*)(tabc + KG_TAB);
        u16 LDSP* fa = (u16 LDSP*)(smem + (2 * chs) * CC_B + XS_B + FPAD * 2);
        {
            const int fr = lane & 15, fg = lane >> 4;
            const char LDSP* yp = tabc + (41 + fr) * KG_ROWB + fg * 16;
            const char LDSP* xp = tabc + (mh * 16 + fr) * KG_ROWB + fg * 16;
            const char LDSP* xq = tabc + (32 + (fr > 8 ? 8 : fr)) * KG_ROWB + fg * 16;
            const bool lo8 = fg < 2;
            const int l0 = mh ? (lo8 ? 256 + fg * 128 : 512 + (fg - 2) * 256) : ((fg >> 1) * 128 + (fg & 1) * 4);
            u16 LDSP* dst = (mh ? kq : fa) + l0 + 8 * fr;
            const int o1 = mh ? (lo8 ? 2 : 4) : 1, o2 = mh ? (lo8 ? 4 : 128) : 2;
            const int lb = 1024 + fg * 512 + 8 * fr;
            u16 LDSP* d20 = kq + (lb <= 2048 ? lb : 2049 + fr);
            u16 LDSP* d21 = kq + (lo8 ? lb + 128 : 1 + 2 * fr);
            const int o21 = lo8 ? 128 : 0;
            lds_wait_ge(SWA(SW_KT + chs), 2);
            const f32x2 sc = *(const f32x2 LDSP*)(smem + SYNC_OFF + (SW_SC + chs * 2) * 4);
            f32x4 d0 = {0.f, 0.f, 0.f, 0.f}, d1 = d0, e0 = d0, e1 = d0;
#pragma unroll
            for (int ks = 0; ks < 4; ++ks) {
                const f16x8 y0 = *(const f16x8 LDSP*)(yp + ks * 64), y1 = *(const f16x8 LDSP*)(yp + 256 + ks * 64);
                d0 = __builtin_amdgcn_mfma_f32_16x16x32_f16(*(const f16x8 LDSP*)(xp + ks * 64), y0, d0, 0, 0, 0);
                d1 = __builtin_amdgcn_mfma_f32_16x16x32_f16(*(const f16x8 LDSP*)(xp + 256 + ks * 64), y1, d1, 0, 0, 0);
                e0 = __builtin_amdgcn_mfma_f32_16x16x32_f16(*(const f16x8 LDSP*)(xq + ks * 64), y0, e0, 0, 0, 0);
                e1 = __builtin_amdgcn_mfma_f32_16x16x32_f16(*(const f16x8 LDSP*)(xq + 256 + ks * 64), y1, e1, 0, 0, 0);
            }
            const f32x4 dv = d0 * sc[0] + d1 * sc[1], ev = e0 * sc[0] + e1 * sc[1];
            const unsigned v01 = cvtpk(dv[0], dv[1]), v23 = cvtpk(dv[2], dv[3]);
            const unsigned w01 = cvtpk(ev[0], ev[1]), w23 = cvtpk(ev[2], ev[3]);
            dst[0] = (u16)v01;
            dst[o1] = (u16)(v01 >> 16);
            dst[o2] = (u16)v23;
            dst[o1 + o2] = (u16)(v23 >> 16);
            d20[0] = (u16)w01;
            d21[0] = (u16)(w01 >> 16);
            d21[o21] = (u16)w23;
            d21[2 * o21] = (u16)(w23 >> 16);
        }
        STAMP(0, 3);
        if (mh == 0) {
#pragma unroll
            for (int h = 0; h < 2; ++h) {
                const int m = 128 + 64 * h + lane;
                const unsigned A = kq[8 * m], B = kq[8 * m + 8], M = kq[8 * m + 8 * (lane & 1)];
                const unsigned AA = A | (A << 16), BB = B | (B << 16);
                *(u32x4 LDSP*)(fa + 8 * m) = u32x4{AA, AA, M | (B << 16), BB};
            }
        } else {
            const int m2 = 64 + lane;
            const unsigned A2 = kq[8 * m2], B2 = kq[8 * m2 + 4], C2 = kq[8 * m2 + 8];
            *(u32x4 LDSP*)(fa + 8 * m2) = u32x4{A2 | (A2 << 16), A2 | (B2 << 16), B2 | (B2 << 16), C2 | (C2 << 16)};
            const int m1 = 32 + (lane & 31);
            const unsigned A1 = kq[8 * m1], B1 = kq[8 * m1 + 2], C1 = kq[8 * m1 + 4], D1 = kq[8 * m1 + 6], E1 = kq[8 * m1 + 8];
            *(u32x4 LDSP*)(fa + 8 * m1) = u32x4{A1 | (A1 << 16), B1 | (C1 << 16), C1 | (C1 << 16), D1 | (E1 << 16)};
        }
        lds_arrive(SWA(SW_FR + 2 * chs));
#pragma unroll
        for (int i = 0; i < 2; ++i)
            *(u32x4 LDSP*)(smem + (2 * chs + 1) * CC_B + XS_B + FPAD * 2 + (mh * 2 + i) * 1024 + lane * 16) = fv[i];
        lds_arrive(SWA(SW_FR + 2 * chs + 1));
        STAMP(0, 4);
        lds_wait_ge(SWA(SW_GO), 1);
        u32x2 gts[4];
        {
            const GAS u64* gp = (const GAS u64*)(ws + WS_G) + kw * 128 + lane * 2;
            unsigned spins = 1u << 18;
            bool again;
#pragma nounroll
            do {
                unsigned bad = 0;
#pragma unroll
                for (int i = 0; i < 4; ++i) {
                    const u64 v0 = __hip_atomic_load(gp + i * 512, RLX_AGENT), v1 = __hip_atomic_load(gp + i * 512 + 1, RLX_AGENT);
                    gts[i] = u32x2{(unsigned)v0, (unsigned)v1};
                    bad |= ((unsigned)(v0 >> 32) ^ (unsigned)tag) | ((unsigned)(v1 >> 32) ^ (unsigned)tag);
                }
                again = __any(bad != 0) && --spins != 0;
            } while (again);
            CBAR();
            if (__builtin_expect(blk == 0, 0)) {
                if (lds_arrive_rtn(SWA(SW_G2)) == 3 && lane == 0) __hip_atomic_store((GAS u64*)(ws + WS_GEN), tag, RLX_AGENT);
            }
        }
        STAMP(0, 5);
        float ds0 = bf_lo(r_ds), ds1 = bf_hi(r_ds), sd0 = bf_lo(r_sd), sd1 = bf_hi(r_sd);
        PIN(ds0); PIN(ds1); PIN(sd0); PIN(sd1);
        const char LDSP* xr = smem + XPAD * 2 + (kw * 128 + lane * 2) * 2;
        const char LDSP* yr = smem + YF_OFF + (kw * 128 + lane * 2) * 4;
        const __amdgpu_buffer_rsrc_t ro = __builtin_amdgcn_make_buffer_rsrc(a.out, 0, L * D * 2, 0x00020000);
        const unsigned oo = ((kw * 128 + lane * 2) * D + cbase) * 2;
        const unsigned pa = SWA(SW_PROG + (lane & 3));
#pragma unroll
        for (int i = 0; i < 4; ++i) {
            lds_wait_all_ge(pa, (i * 4 + kw) * 2 + 2);
            const f32x2 ya0 = *(const f32x2 LDSP*)(yr + i * 2048), yb0 = *(const f32x2 LDSP*)(yr + YF_B + i * 2048);
            const f32x2 ya1 = *(const f32x2 LDSP*)(yr + 2 * YF_B + i * 2048), yb1 = *(const f32x2 LDSP*)(yr + 3 * YF_B + i * 2048);
            const f32x2 u0 = bf_pair(*(const unsigned LDSP*)(xr + i * 1024)), k0 = bf_pair(*(const unsigned LDSP*)(xr + CC_B + i * 1024));
            const f32x2 u1 = bf_pair(*(const unsigned LDSP*)(xr + 2 * CC_B + i * 1024)), k1 = bf_pair(*(const unsigned LDSP*)(xr + 3 * CC_B + i * 1024));
            const float g0 = bf_lo(gts[i][0]), h0 = bf_hi(gts[i][0]), g1 = bf_lo(gts[i][1]), h1 = bf_hi(gts[i][1]);
            const float o00 = epi1(ya0[0], yb0[0], u0[0], k0[0], g0, h0, ds0, sd0);
            const float o10 = epi1(ya1[0], yb1[0], u1[0], k1[0], g0, h0, ds1, sd1);
            const float o01 = epi1(ya0[1], yb0[1], u0[1], k0[1], g1, h1, ds0, sd0);
            const float o11 = epi1(ya1[1], yb1[1], u1[1], k1[1], g1, h1, ds1, sd1);
            __builtin_amdgcn_raw_buffer_store_b32(cvtpk(o00, o10), ro, oo, i * 512 * D * 2, 0);
            __builtin_amdgcn_raw_buffer_store_b32(cvtpk(o01, o11), ro, oo + D * 2, i * 512 * D * 2, 0);
            STAMP(0, 8 + i);
        }
        STAMP(0, 6);
    }
}

extern "C" void kernel_launch(void* const* d_in, const int* in_sizes, int n_in, void* d_out, int out_size, void* d_ws,
                              size_t ws_size, hipStream_t stream) {
    Args a;
    a.u = (const u16*)d_in[0];
    a.Are = (const u16*)d_in[1];
    a.Aim = (const u16*)d_in[2];
    a.Bre = (const u16*)d_in[3];
    a.Bim = (const u16*)d_in[4];
    a.Cre = (const u16*)d_in[5];
    a.Cim = (const u16*)d_in[6];
    a.logdt = (const u16*)d_in[7];
    a.dskip = (const u16*)d_in[8];
    a.kk = (const u16*)d_in[10];
    a.vv = (const u16*)d_in[11];
    a.shk = (const u16*)d_in[12];
    a.ssmk = (const u16*)d_in[13];
    a.ssmd = (const u16*)d_in[14];
    a.gw = (const u16*)d_in[15];
    a.out = (u16*)d_out;
    a.ws = (char*)d_ws;
    (void)hipFuncSetAttribute((const void*)fused_kernel, hipFuncAttributeMaxDynamicSharedMemorySize, LDS_BYTES);
    fused_kernel<<<256, 512, LDS_BYTES, stream>>>(a);
}
```

```cpp
#include <hip/hip_runtime.h>
#include <hip/hip_bf16.h>
#include <stdint.h>

typedef unsigned short u16;
typedef unsigned long long u64;
typedef short s16x4 __attribute__((ext_vector_type(4)));
typedef short s16x8 __attribute__((ext_vector_type(8)));
typedef _Float16 f16x8 __attribute__((ext_vector_type(8)));
typedef float f32x4 __attribute__((ext_vector_type(4)));
typedef float f32x2 __attribute__((ext_vector_type(2)));
typedef unsigned u32x4 __attribute__((ext_vector_type(4)));
typedef unsigned u32x2 __attribute__((ext_vector_type(2)));
#define LDSP __attribute__((address_space(3)))
#define GAS __attribute__((address_space(1)))
#define RLX_AGENT __ATOMIC_RELAXED, __HIP_MEMORY_SCOPE_AGENT
#define RLX_WG __ATOMIC_RELAXED, __HIP_MEMORY_SCOPE_WORKGROUP
#define SBAR() __builtin_amdgcn_sched_barrier(0)
#define CBAR() asm volatile("" ::: "memory")
#define PIN(x) asm volatile("" : "+v"(x))
#define LD(p) (*(p))
constexpr int SC1 = 16;

constexpr int L = 2048, D = 512, NM = 128;

constexpr unsigned WS_XT = 0;
constexpr unsigned WS_G = WS_XT + 2u * D * L * 2;
constexpr unsigned WS_FL = WS_G + L * 8;
constexpr unsigned WS_GEN = WS_FL + 256 * 128;
constexpr unsigned WS_USED = WS_GEN + 256;
constexpr u64 TAG_INC = 0x9E3779B97F4A7C15ull;
constexpr unsigned CODE_PF = 17408;

constexpr int XPAD = 128, FPAD = 16;
constexpr int XS_B = (XPAD + L) * 2;
constexpr int FS_B = (FPAD + L) * 2;
constexpr int CC_B = XS_B + FS_B;
constexpr int KG_OFF = 0;
constexpr int KG_ROWB = 528;
constexpr int KG_TAB = 57 * KG_ROWB;
constexpr int KG_Q = 4224;
constexpr int KG_CH = KG_TAB + KG_Q;
constexpr int YF_OFF = 2 * KG_CH;
constexpr int YF_B = L * 4 + 16;
constexpr int CC_OFF = YF_OFF + 4 * YF_B;
constexpr int TS_OFF = CC_OFF + 4 * CC_B;
constexpr int SYNC_OFF = TS_OFF + 16384;
constexpr int LDS_BYTES = SYNC_OFF + 128;
enum { SW_PROG = 0, SW_T1 = 4, SW_T2 = 5, SW_KT = 6, SW_FR = 8, SW_GO = 12, SW_TAG = 14, SW_G2 = 16, SW_TU = 17 };

__device__ __forceinline__ float bf_lo(unsigned x) { return __uint_as_float(x << 16); }
__device__ __forceinline__ float bf_hi(unsigned x) { return __uint_as_float(x & 0xffff0000u); }
__device__ __forceinline__ float bf2f(u16 x) { return __uint_as_float(((unsigned)x) << 16); }
__device__ __forceinline__ f32x2 bf_pair(unsigned x) { return f32x2{bf_lo(x), bf_hi(x)}; }
__device__ __forceinline__ unsigned cvtpk(float lo, float hi) {
    typedef __bf16 bf16x2 __attribute__((ext_vector_type(2)));
    f32x2 v = {lo, hi};
    bf16x2 r = __builtin_convertvector(v, bf16x2);
    return __builtin_bit_cast(unsigned, r);
}
__device__ __forceinline__ unsigned cvtpk_h(f32x2 v) {
    typedef _Float16 f16x2 __attribute__((ext_vector_type(2)));
    f16x2 r = __builtin_convertvector(v, f16x2);
    return __builtin_bit_cast(unsigned, r);
}
__device__ __forceinline__ float rbf(float x) { return bf_lo(cvtpk(x, x)); }
__device__ __forceinline__ float addbf(float a, float b, float c) {
    typedef __bf16 bf16x2 __attribute__((ext_vector_type(2)));
    const bf16x2 one = {(__bf16)1.0f, (__bf16)1.0f};
    return __builtin_amdgcn_fdot2_f32_bf16(__builtin_bit_cast(bf16x2, cvtpk(a, b)), one, c, false);
}
__device__ __forceinline__ void chain4(f32x2 a3, f32x2 a2, f32x2 a1, f32x2 a0, f32x2 b3, f32x2 b2, f32x2 b1, f32x2 b0,
                                       unsigned& oa, unsigned& ob) {
    unsigned t0, t1, t2, t3; float s0, s1, s2, s3;
#define DOT4 "v_dot2_f32_bf16 %6, %2, %26, 0\n\tv_dot2_f32_bf16 %7, %3, %26, 0\n\tv_dot2_f32_bf16 %8, %4, %26, 0\n\tv_dot2_f32_bf16 %9, %5, %26, 0\n\t"
    asm("v_cvt_pk_bf16_f32 %2, %10, %12\n\tv_cvt_pk_bf16_f32 %3, %11, %13\n\tv_cvt_pk_bf16_f32 %4, %18, %20\n\tv_cvt_pk_bf16_f32 %5, %19, %21\n\t"
        DOT4
        "v_cvt_pk_bf16_f32 %2, %6, %14\n\tv_cvt_pk_bf16_f32 %3, %7, %15\n\tv_cvt_pk_bf16_f32 %4, %8, %22\n\tv_cvt_pk_bf16_f32 %5, %9, %23\n\t"
        DOT4
        "v_cvt_pk_bf16_f32 %2, %6, %16\n\tv_cvt_pk_bf16_f32 %3, %7, %17\n\tv_cvt_pk_bf16_f32 %4, %8, %24\n\tv_cvt_pk_bf16_f32 %5, %9, %25\n\t"
        DOT4
        "s_nop 1\n\t"
        "v_cvt_pk_bf16_f32 %0, %6, %7\n\tv_cvt_pk_bf16_f32 %1, %8, %9"
        : "=&v"(oa), "=&v"(ob), "=&v"(t0), "=&v"(t1), "=&v"(t2), "=&v"(t3), "=&v"(s0), "=&v"(s1), "=&v"(s2), "=&v"(s3)
        : "v"(a3[0]), "v"(a3[1]), "v"(a2[0]), "v"(a2[1]), "v"(a1[0]), "v"(a1[1]), "v"(a0[0]), "v"(a0[1]),
          "v"(b3[0]), "v"(b3[1]), "v"(b2[0]), "v"(b2[1]), "v"(b1[0]), "v"(b1[1]), "v"(b0[0]), "v"(b0[1]), "s"(0x3f803f80u));
#undef DOT4
}
__device__ __forceinline__ float exp_bf(float x) {
    const float LH = 1.44268798828125f;
    const float LL = (float)((1.4426950408889634 - 1.44268798828125) * 0.6931471805599453);
    const float r = __builtin_amdgcn_exp2f(x * LH);
    const float c = x * LL;
    return __builtin_fmaf(r, __builtin_fmaf(0.5f * c, c, c), r);
}
__device__ __forceinline__ float epi1(float ya, float yb, float u, float kv, float g, float omg, float ds, float sd) {
    float a, b;
    asm("v_fma_f32 %0, %4, %8, %2\n\t"
        "v_fma_f32 %1, %5, %9, %3\n\t"
        "v_mul_f32 %0, %6, %0\n\t"
        "v_mul_f32 %1, %4, %1\n\t"
        "v_fma_f32 %0, %7, %1, %0"
        : "=&v"(a), "=&v"(b) : "v"(ya), "v"(yb), "v"(u), "v"(kv), "v"(g), "v"(omg), "v"(ds), "v"(sd));
    return a;
}

__device__ __forceinline__ void lds_wait_ge(unsigned addr, unsigned need) {
    unsigned v, s, cnt;
    asm volatile("s_mov_b32 %2, 0x400000\n"
                 "1:\n\t"
                 "ds_read_b32 %0, %3\n\t"
                 "s_waitcnt lgkmcnt(0)\n\t"
                 "v_readfirstlane_b32 %1, %0\n\t"
                 "s_sub_u32 %2, %2, 1\n\t"
                 "s_cselect_b32 %1, -1, %1\n\t"
                 "s_cmp_ge_u32 %1, %4\n\t"
                 "s_cbranch_scc0 1b"
                 : "=&v"(v), "=&s"(s), "=&s"(cnt) : "v"(addr), "s"(need) : "memory", "scc");
}
__device__ __forceinline__ void lds_wait_all_ge(unsigned addr, unsigned need) {
    unsigned v, cnt;
    asm volatile("s_mov_b32 %1, 0x400000\n"
                 "1:\n\t"
                 "ds_read_b32 %0, %2\n\t"
                 "s_waitcnt lgkmcnt(0)\n\t"
                 "v_cmp_gt_u32 vcc, %3, %0\n\t"
                 "s_sub_u32 %1, %1, 1\n\t"
                 "s_cselect_b64 vcc, 0, vcc\n\t"
                 "s_cbranch_vccnz 1b"
                 : "=&v"(v), "=&s"(cnt) : "v"(addr), "s"(need) : "memory", "scc", "vcc");
}
__device__ __forceinline__ void lds_arrive(unsigned addr) {
    u64 sv; unsigned one = 1;
    asm volatile("s_mov_b64 %0, exec\n\t"
                 "s_mov_b64 exec, 1\n\t"
                 "ds_add_u32 %1, %2\n\t"
                 "s_mov_b64 exec, %0"
                 : "=&s"(sv) : "v"(addr), "v"(one) : "memory");
}
__device__ __forceinline__ unsigned lds_arrive_rtn(unsigned addr) {
    u64 sv; unsigned one = 1, r, s;
    asm volatile("s_mov_b64 %0, exec\n\t"
                 "s_mov_b64 exec, 1\n\t"
                 "ds_add_rtn_u32 %1, %3, %4\n\t"
                 "s_waitcnt lgkmcnt(0)\n\t"
                 "v_readfirstlane_b32 %2, %1\n\t"
                 "s_mov_b64 exec, %0"
                 : "=&s"(sv), "=&v"(r), "=&s"(s) : "v"(addr), "v"(one) : "memory");
    return s;
}

template <int CTRL, int RMASK>
__device__ __forceinline__ float dpp_add(float x) {
    return x + __int_as_float(__builtin_amdgcn_update_dpp(0, __float_as_int(x), CTRL, RMASK, 0xf, true));
}
__device__ __forceinline__ float wave_sum63(float x) {
    x = dpp_add<0x111, 0xf>(x);
    x = dpp_add<0x112, 0xf>(x);
    x = dpp_add<0x114, 0xf>(x);
    x = dpp_add<0x118, 0xf>(x);
    x = dpp_add<0x142, 0xa>(x);
    x = dpp_add<0x143, 0xc>(x);
    return x;
}
template <int CTRL, int RMASK>
__device__ __forceinline__ unsigned dpp_maxu(unsigned x) {
    return max(x, (unsigned)__builtin_amdgcn_update_dpp(0, (int)x, CTRL, RMASK, 0xf, true));
}
__device__ __forceinline__ unsigned wave_maxu(unsigned x) {
    x = dpp_maxu<0x111, 0xf>(x);
    x = dpp_maxu<0x112, 0xf>(x);
    x = dpp_maxu<0x114, 0xf>(x);
    x = dpp_maxu<0x118, 0xf>(x);
    x = dpp_maxu<0x142, 0xa>(x);
    x = dpp_maxu<0x143, 0xc>(x);
    return (unsigned)__builtin_amdgcn_readlane((int)x, 63);
}

typedef f32x2 cf;
__device__ __forceinline__ cf cmul(cf a, cf b) {
    cf r;
    asm("v_pk_mul_f32 %0, %1, %2 op_sel_hi:[0,1]\n\t"
        "v_pk_fma_f32 %0, %1, %2, %0 op_sel:[1,1,0] op_sel_hi:[1,0,1] neg_lo:[0,1,0]"
        : "=&v"(r) : "v"(a), "v"(b));
    return r;
}
__device__ __forceinline__ cf cexp_p2(float lr, float fh, float fl, float l) {
    const float mag = __builtin_amdgcn_exp2f(lr * l);
    const float ph = __builtin_amdgcn_fractf(fh * l) + fl * l;
    return cf{mag * __builtin_amdgcn_cosf(ph), mag * __builtin_amdgcn_sinf(ph)};
}
__device__ __forceinline__ s16x4 trread(const char LDSP* p) {
    return __builtin_amdgcn_ds_read_tr16_b64_v4i16((s16x4 LDSP*)p);
}

template <int V> struct ic { static constexpr int value = V; };
template <int B, int E, class F>
__device__ __forceinline__ void static_for(F&& f) {
    if constexpr (B < E) { f(ic<B>{}); static_for<B + 1, E>(f); }
}

constexpr int tile_start(int T) { int s = 0; for (int i = 0; i < T; ++i) s += (i >> 1) + 1; return s; }
constexpr int NMFMA = tile_start(32);
struct Sched { short xr[NMFMA + 64]; short fin[NMFMA + 64]; int nslot; };
constexpr Sched make_sched() {
    Sched S{};
    for (int i = 0; i < NMFMA + 64; ++i) { S.xr[i] = -1; S.fin[i] = -1; }
    for (int T = 8; T < 32; ++T) { S.xr[tile_start(T - 2)] = (short)(2 * T); S.xr[tile_start(T - 2) + 1] = (short)(2 * T + 1); }
    int g = 0;
    for (int T = 0; T < 32; ++T) {
        int first = tile_start(T + 1) - 1 + 4;
        if (first > NMFMA) first = NMFMA;
        if (g < first) g = first;
        for (int w = 0; w < ((T & 1) ? 8 : 7); ++w) S.fin[g++] = (short)(T * 8 + w);
    }
    S.nslot = g;
    return S;
}
constexpr Sched SCHED = make_sched();

struct Args {
    const u16 *u, *kk, *vv, *shk, *gw, *Are, *Aim, *Bre, *Bim, *Cre, *Cim, *logdt, *dskip, *ssmk, *ssmd;
    u16* out;
    char* ws;
};

__global__ __launch_bounds__(512, 2) void fused_kernel(Args a) {
    extern __shared__ __attribute__((aligned(16))) char smem_g[];
    char LDSP* smem = (char LDSP*)smem_g;
    unsigned cpf;
    {
        u64 pc;
        unsigned off = threadIdx.x * 128u;
        off = off < CODE_PF ? off : 0u;
        asm volatile("s_getpc_b64 %1\n\tglobal_load_dword %0, %2, %1" : "=v"(cpf), "=&s"(pc) : "v"(off) : "memory");
    }
#define CPF_DONE() asm volatile("" :: "v"(cpf))
    char* ws = a.ws;
    const int tid = threadIdx.x, lane = tid & 63;
    const int wv = __builtin_amdgcn_readfirstlane(tid >> 6);
    const int blk = blockIdx.x;
    const int cb = blk & 7, tb = blk >> 3;
    const int cbase = cb * 64 + tb * 2;
    const unsigned swb = (unsigned)(uintptr_t)(smem + SYNC_OFF);
#define SWA(i) (swb + 4u * (unsigned)(i))
    const __amdgpu_buffer_rsrc_t rs = __builtin_amdgcn_make_buffer_rsrc(ws, 0, WS_USED, 0x00020000);
    asm volatile("" :: "s"(a.u), "s"(a.kk), "s"(a.vv), "s"(a.shk), "s"(a.gw), "s"(a.Are), "s"(a.Aim), "s"(a.Bre), "s"(a.Bim));
    asm volatile("" :: "s"(a.Cre), "s"(a.Cim), "s"(a.logdt), "s"(a.dskip), "s"(a.ssmk), "s"(a.ssmd), "s"(a.out), "s"(a.ws));

    if (wv >= 4) {
        __builtin_amdgcn_s_setprio(1);
        const int kw = wv - 4;
        const int chs = kw >> 1, mh = kw & 1;
        const int c = cbase + chs;
        const int n = mh * 64 + lane;
        const int pi = c * NM + n;
        const u16 r_are = LD(a.Are + pi), r_aim = LD(a.Aim + pi), r_bre = LD(a.Bre + pi), r_bim = LD(a.Bim + pi), r_cre = LD(a.Cre + pi), r_cim = LD(a.Cim + pi);
        const u16 r_ldt = LD(a.logdt + c);
        SBAR();
        asm volatile("s_barrier" ::: "memory");
        SBAR();
        const float dt = rbf(exp_bf(bf2f(r_ldt)));
        const float ar = dt * bf2f(r_are), ai = dt * bf2f(r_aim);
        cf CB = cmul(cf{bf2f(r_cre), bf2f(r_cim)}, cf{dt * bf2f(r_bre), dt * bf2f(r_bim)});
        const float lr = ar * 1.4426950408889634f;
        const float I2PH = 0.15915494309189535f;
        const float I2PL = (float)(0.15915494309189535 - (double)0.15915494309189535f);
        const float fh = ai * I2PH;
        const float fl = __builtin_fmaf(ai, I2PH, -fh) + ai * I2PL;
        int ex, ey;
        {
            const unsigned mcb = wave_maxu(max(__float_as_uint(CB[0]) & 0x7fffffffu, __float_as_uint(CB[1]) & 0x7fffffffu));
            int ecb = (int)(mcb >> 23) - 127;
            ecb = ecb < -96 ? -96 : ecb;
            int gx = 0, gy = 0;
            if (__builtin_expect(__any(lr > 0.f), 0)) {
                const float grow = __uint_as_float(wave_maxu(__float_as_uint(fmaxf(lr, 0.f))));
                gx = (int)fminf(ceilf(grow * 2048.f), 120.f);
                gy = (int)fminf(ceilf(grow * 120.f), 60.f);
            }
            ex = 13 - ecb - gx;
            ex = ex < -80 ? -80 : ex;
            ey = 14 - gy;
        }
        CB *= __uint_as_float((unsigned)(ex + 127) << 23);
        CPF_DONE();

        char LDSP* tabc = smem + KG_OFF + chs * KG_CH;
        asm volatile("s_mov_b32 m0, %0\n\ts_nop 0" :: "s"((unsigned)(uintptr_t)tabc + mh * 256) : "memory");
#define PUT(row, v) asm volatile("ds_write_addtid_b32 %0 offset:%1" :: "v"(cvtpk_h(v)), "i"((row) * KG_ROWB) : "memory")
        const int fr = lane & 15, fg = lane >> 4;
        const char LDSP* own = tabc + mh * 256 + fg * 16;
        const char LDSP* yp = own + (41 + fr) * KG_ROWB;
        const char LDSP* xp = own + fr * KG_ROWB;
        const char LDSP* xq = own + (32 + (fr > 8 ? 8 : fr)) * KG_ROWB;
        f16x8 yfr[4];
#define KTILE(dst, ptr)                                                                                   \
    do {                                                                                                  \
        dst = f32x4{0.f, 0.f, 0.f, 0.f};                                                                  \
        _Pragma("unroll") for (int ks = 0; ks < 4; ++ks)                                                  \
            dst = __builtin_amdgcn_mfma_f32_16x16x32_f16(*(const f16x8 LDSP*)((ptr) + ks * 64), yfr[ks], dst, 0, 0, 0); \
    } while (0)
        f32x4 d0, d1, d2;
        {
            const float sy = __uint_as_float((unsigned)(ey + 127) << 23);
            const cf q0 = cexp_p2(lr, -fh, -fl, 8.f), q1 = cmul(q0, q0), q2 = cmul(q1, q1), q3 = cmul(q2, q2);
            cf y[8];
            y[0] = cf{sy, 0.f};
            y[1] = q0 * sy;
            y[2] = q1 * sy;
            y[4] = q2 * sy;
            y[3] = cmul(y[1], q1);
            y[5] = cmul(y[1], q2);
            y[6] = cmul(y[2], q2);
            y[7] = cmul(y[3], q2);
#pragma unroll
            for (int i = 0; i < 8; ++i) PUT(41 + i, y[i]);
            PUT(49, q3 * sy);
#pragma unroll
            for (int i = 1; i < 8; ++i) PUT(49 + i, cmul(y[i], q3));
        }
#pragma unroll
        for (int ks = 0; ks < 4; ++ks) yfr[ks] = *(const f16x8 LDSP*)(yp + ks * 64);
        {
            const cf p1 = cexp_p2(lr, fh, fl, 128.f), p2 = cmul(p1, p1), p4 = cmul(p2, p2), p8 = cmul(p4, p4), p16 = cmul(p8, p8);
            cf G[8];
            G[0] = CB;
            G[1] = cmul(G[0], p1);
            G[2] = cmul(G[0], p2);
            G[3] = cmul(G[1], p2);
#pragma unroll
            for (int i = 0; i < 4; ++i) G[4 + i] = cmul(G[i], p4);
#pragma unroll
            for (int gg = 0; gg < 8; ++gg) PUT(32 + gg, cmul(G[gg], p8));
            PUT(40, cmul(G[0], p16));
            KTILE(d2, xq);
            cf e[8];
            e[1] = cexp_p2(lr, fh, fl, 1.f);
            e[2] = cmul(e[1], e[1]);
            e[4] = cmul(e[2], e[2]);
            e[3] = cmul(e[1], e[2]);
            e[5] = cmul(e[1], e[4]);
            e[6] = cmul(e[2], e[4]);
            e[7] = cmul(e[3], e[4]);
#pragma unroll
            for (int gg = 0; gg < 2; ++gg) {
                PUT(8 * gg, G[gg]);
#pragma unroll
                for (int r = 1; r < 8; ++r) PUT(8 * gg + r, cmul(G[gg], e[r]));
            }
            KTILE(d0, xp);
#pragma unroll
            for (int gg = 2; gg < 4; ++gg) {
                PUT(16 + 4 * (gg - 2), G[gg]);
#pragma unroll
                for (int r = 1; r < 4; ++r) PUT(16 + 4 * (gg - 2) + r, cmul(G[gg], e[2 * r]));
            }
#pragma unroll
            for (int gg = 4; gg < 8; ++gg) {
                PUT(24 + 2 * (gg - 4), G[gg]);
                PUT(24 + 2 * (gg - 4) + 1, cmul(G[gg], e[4]));
            }
            KTILE(d1, xp + 16 * KG_ROWB);
        }

        u32x4 fv[2];
#pragma unroll
        for (int i = 0; i < 2; ++i) fv[i] = LD((const u32x4*)(a.ssmk + (size_t)c * L + (mh * 2 + i) * 512 + lane * 8));
        const unsigned r_ds = LD((const unsigned*)(a.dskip + cbase)), r_sd = LD((const unsigned*)(a.ssmd + cbase));
        u16 LDSP* kq = (u16 LDSP*)(tabc + KG_TAB);
        u16 LDSP* fa = (u16 LDSP*)(smem + CC_OFF + (2 * chs) * CC_B + XS_B + FPAD * 2);
        {
            const float sc = __uint_as_float((unsigned)(127 - ex - ey) << 23);
            const f32x4 t2 = d2 * sc;
            const int xo_ = (41 + fg) * KG_ROWB + fr * 16;
            *(f32x4 LDSP*)(tabc + mh * 256 + xo_) = d0 * sc;
            *(f32x4 LDSP*)(tabc + mh * 256 + xo_ + 4 * KG_ROWB) = d1 * sc;
            *(f32x4 LDSP*)(tabc + mh * 256 + xo_ + 8 * KG_ROWB) = t2;
            lds_arrive(SWA(SW_KT + chs));
            const unsigned sh = fg * 8;
            const unsigned l0 = 4 * __builtin_amdgcn_ubfe(mh ? 0xC0806040u : 0x21200100u, sh, 8);
            const unsigned o1 = __builtin_amdgcn_ubfe(mh ? 0x04040202u : 0x01010101u, sh, 8);
            const unsigned o2 = __builtin_amdgcn_ubfe(mh ? 0x80800404u : 0x02020202u, sh, 8);
            u16 LDSP* dst = (u16 LDSP*)(smem + (mh ? KG_OFF + chs * KG_CH + KG_TAB : CC_OFF + (2 * chs) * CC_B + XS_B + FPAD * 2)) + l0 + 8 * fr;
            const unsigned lb = 1024 + fg * 512 + 8 * fr;
            lds_wait_ge(SWA(SW_KT + chs), 2);
            const char LDSP* rp = tabc + xo_ + mh * 4 * KG_ROWB;
            const f32x4 dv = *(const f32x4 LDSP*)rp + *(const f32x4 LDSP*)(rp + 256);
            const f32x4 ev = t2 + *(const f32x4 LDSP*)(tabc + (mh ^ 1) * 256 + xo_ + 8 * KG_ROWB);
            const unsigned v01 = cvtpk(dv[0], dv[1]), v23 = cvtpk(dv[2], dv[3]);
            const unsigned w01 = cvtpk(ev[0], ev[1]), w23 = cvtpk(ev[2], ev[3]);
            dst[0] = (u16)v01;
            dst[o1] = (u16)(v01 >> 16);
            dst[o2] = (u16)v23;
            dst[o1 + o2] = (u16)(v23 >> 16);
            kq[min(lb, 2056u)] = (u16)w01;
            kq[min(lb + 128, 2056u)] = (u16)(w01 >> 16);
            kq[min(lb + 256, 2056u)] = (u16)w23;
            kq[min(lb + 384, 2056u)] = (u16)(w23 >> 16);
        }

        if (mh == 0) {
            const u16 LDSP* q = kq + 8 * (128 + lane);
            const int om = 8 * (lane & 1);
            const unsigned A = q[0], B = q[8], M = q[om], A2 = q[512], B2 = q[520], M2 = q[512 + om];
            const unsigned AA = A | (A << 16), AA2 = A2 | (A2 << 16);
            *(u32x4 LDSP*)(fa + 8 * (128 + lane)) = u32x4{AA, AA, M | (B << 16), B | (B << 16)};
            *(u32x4 LDSP*)(fa + 8 * (192 + lane)) = u32x4{AA2, AA2, M2 | (B2 << 16), B2 | (B2 << 16)};
        } else {
            const int m2 = 64 + lane, m1 = 32 + (lane & 31);
            const unsigned A2 = kq[8 * m2], B2 = kq[8 * m2 + 4], C2 = kq[8 * m2 + 8];
            const unsigned A1 = kq[8 * m1], B1 = kq[8 * m1 + 2], C1 = kq[8 * m1 + 4], D1 = kq[8 * m1 + 6], E1 = kq[8 * m1 + 8];
            *(u32x4 LDSP*)(fa + 8 * m2) = u32x4{A2 | (A2 << 16), A2 | (B2 << 16), B2 | (B2 << 16), C2 | (C2 << 16)};
            *(u32x4 LDSP*)(fa + 8 * m1) = u32x4{A1 | (A1 << 16), B1 | (C1 << 16), C1 | (C1 << 16), D1 | (E1 << 16)};
        }
        lds_arrive(SWA(SW_FR + 2 * chs));
#pragma unroll
        for (int i = 0; i < 2; ++i)
            *(u32x4 LDSP*)(smem + CC_OFF + (2 * chs + 1) * CC_B + XS_B + FPAD * 2 + (mh * 2 + i) * 1024 + lane * 16) = fv[i];
        lds_arrive(SWA(SW_FR + 2 * chs + 1));

        __builtin_amdgcn_s_setprio(0);
        lds_wait_ge(SWA(SW_GO), 1);
        const u32x2 tagw = *(const u32x2 LDSP*)(smem + SYNC_OFF + SW_TAG * 4);
        const u64 tag = ((u64)tagw[1] << 32) | tagw[0];
        unsigned gts[4];
        {
            const GAS u64* gp = (const GAS u64*)(ws + WS_G) + kw * 64 + lane;
            unsigned spins = 1u << 18;
            bool again;
#pragma nounroll
            do {
                unsigned bad = 0;
#pragma unroll
                for (int i = 0; i < 4; ++i) {
                    const u64 v0 = __hip_atomic_load(gp + i * 256, RLX_AGENT);
                    gts[i] = (unsigned)v0;
                    bad |= (unsigned)(v0 >> 32) ^ (unsigned)tag;
                }
                again = __any(bad != 0) && --spins != 0;
            } while (again);
            CBAR();
            if (__builtin_expect(blk == 0, 0)) {
                if (lds_arrive_rtn(SWA(SW_G2)) == 3 && lane == 0) __hip_atomic_store((GAS u64*)(ws + WS_GEN), tag, RLX_AGENT);
            }
        }

        float ds0 = bf_lo(r_ds), ds1 = bf_hi(r_ds), sd0 = bf_lo(r_sd), sd1 = bf_hi(r_sd);
        PIN(ds0); PIN(ds1); PIN(sd0); PIN(sd1);
        const char LDSP* xr = smem + CC_OFF + XPAD * 2 + (kw * 128 + lane * 2) * 2;
        const char LDSP* yr = smem + YF_OFF + (kw * 128 + lane * 2) * 4;
        const __amdgpu_buffer_rsrc_t ro = __builtin_amdgcn_make_buffer_rsrc(a.out, 0, L * D * 2, 0x00020000);
        const unsigned oo = ((kw * 128 + lane * 2) * D + cbase) * 2;
        const unsigned pa = SWA(SW_PROG + (lane & 3));
#pragma unroll
        for (int i = 0; i < 4; ++i) {
            lds_wait_all_ge(pa, (i * 4 + kw) * 2 + 2);
            const f32x2 ya0 = *(const f32x2 LDSP*)(yr + i * 2048), yb0 = *(const f32x2 LDSP*)(yr + YF_B + i * 2048);
            const f32x2 ya1 = *(const f32x2 LDSP*)(yr + 2 * YF_B + i * 2048), yb1 = *(const f32x2 LDSP*)(yr + 3 * YF_B + i * 2048);
            const f32x2 u0 = bf_pair(*(const unsigned LDSP*)(xr + i * 1024)), k0 = bf_pair(*(const unsigned LDSP*)(xr + CC_B + i * 1024));
            const f32x2 u1 = bf_pair(*(const unsigned LDSP*)(xr + 2 * CC_B + i * 1024)), k1 = bf_pair(*(const unsigned LDSP*)(xr + 3 * CC_B + i * 1024));
            const float g0 = bf_lo(gts[i]), g1 = bf_hi(gts[i]);
            float m0 = 1.0f - g0, m1 = 1.0f - g1;
            PIN(m0); PIN(m1);
            const unsigned og = cvtpk(m0, m1);
            const float h0 = bf_lo(og), h1 = bf_hi(og);
            const float o00 = epi1(ya0[0], yb0[0], u0[0], k0[0], g0, h0, ds0, sd0);
            const float o10 = epi1(ya1[0], yb1[0], u1[0], k1[0], g0, h0, ds1, sd1);
            const float o01 = epi1(ya0[1], yb0[1], u0[1], k0[1], g1, h1, ds0, sd0);
            const float o11 = epi1(ya1[1], yb1[1], u1[1], k1[1], g1, h1, ds1, sd1);
            __builtin_amdgcn_raw_buffer_store_b32(cvtpk(o00, o10), ro, oo, i * 512 * D * 2, 0);
            __builtin_amdgcn_raw_buffer_store_b32(cvtpk(o01, o11), ro, oo + D * 2, i * 512 * D * 2, 0);
        }
    } else {
        __builtin_amdgcn_s_setprio(2);
        const int cg = tid & 7, rg = tid >> 3;
        const int c0 = cb * 64 + cg * 8;
        const int t0 = tb * 64 + rg * 2;
        u32x4 ur[2], vr[2], kr[5], sk[4];
#pragma unroll
        for (int i = 0; i < 2; ++i) ur[i] = LD((const u32x4*)(a.u + (size_t)(t0 + i) * D + c0));
        SBAR();
        {
            const __amdgpu_buffer_rsrc_t rk = __builtin_amdgcn_make_buffer_rsrc((void*)a.kk, 0, L * D * 2, 0x00020000);
            const unsigned ko = (unsigned)(((t0 - 3) * D + c0) * 2);
#pragma unroll
            for (int i = 0; i < 5; ++i) kr[i] = __builtin_amdgcn_raw_buffer_load_b128(rk, ko + (unsigned)(i * D * 2), 0, 0);
        }
#pragma unroll
        for (int i = 0; i < 4; ++i) sk[i] = LD((const u32x4*)(a.shk + (size_t)c0 * 4 + i * 8));
#pragma unroll
        for (int i = 0; i < 2; ++i) vr[i] = LD((const u32x4*)(a.vv + (size_t)(t0 + i) * D + c0));
        SBAR();
        const int gr = blk * 8 + wv * 2;
        u32x4 gx[2];
        const u32x4 gwv = LD((const u32x4*)(a.gw + lane * 8));
        const int conv = wv & 1, c = cbase + (wv >> 1);
        char LDSP* xs = smem + CC_OFF + wv * CC_B;
        char LDSP* fs = xs + XS_B;
        {
            const u32x4 z = {0u, 0u, 0u, 0u};
            char LDSP* zp = lane < 16 ? xs + lane * 16 : (lane < 18 ? fs + (lane - 16) * 16 : smem + SYNC_OFF + (lane & 7) * 16);
            *(u32x4 LDSP*)zp = z;
        }
        const u32x2 genw = __builtin_amdgcn_raw_buffer_load_b64(rs, WS_GEN, 0, 0);
        SBAR();
        asm volatile("s_waitcnt lgkmcnt(0)\n\ts_barrier" ::: "memory");
        SBAR();

        unsigned LDSP* lu = (unsigned LDSP*)(smem + TS_OFF);
        unsigned LDSP* lkv = lu + 64 * 32;
        const int col = cg * 8 * 32 + (rg ^ (cg << 2));
#pragma unroll
        for (int h = 0; h < 4; ++h) {
            lu[col + (2 * h) * 32] = __builtin_amdgcn_perm(ur[1][h], ur[0][h], 0x05040100u);
            lu[col + (2 * h + 1) * 32] = __builtin_amdgcn_perm(ur[1][h], ur[0][h], 0x07060302u);
        }
        lds_arrive(SWA(SW_TU));
        asm volatile("s_waitcnt vmcnt(4)" ::: "memory");
        SBAR();
#pragma unroll
        for (int i = 0; i < 2; ++i) gx[i] = LD((const u32x4*)(a.u + (size_t)(gr + i) * D + lane * 8));
        SBAR();
        const int sg = tid & 7, chl = tid >> 3;
#pragma unroll
        for (int h = 0; h < 4; ++h) {
            const f32x2 W0 = {bf_lo(sk[h][0]), bf_lo(sk[h][2])}, W1 = {bf_hi(sk[h][0]), bf_hi(sk[h][2])};
            const f32x2 W2 = {bf_lo(sk[h][1]), bf_lo(sk[h][3])}, W3 = {bf_hi(sk[h][1]), bf_hi(sk[h][3])};
            f32x2 K[5];
#pragma unroll
            for (int i = 0; i < 5; ++i) K[i] = bf_pair(kr[i][h]);
            unsigned pa, pb;
            chain4(K[0] * W3, K[1] * W2, K[2] * W1, K[3] * W0, K[1] * W3, K[2] * W2, K[3] * W1, K[4] * W0, pa, pb);
            const f32x2 sa = bf_pair(pa), sb = bf_pair(pb);
            const f32x2 ka = sa * bf_pair(vr[0][h]);
            const f32x2 kb = sb * bf_pair(vr[1][h]);
            lkv[col + (2 * h) * 32] = cvtpk(ka[0], kb[0]);
            lkv[col + (2 * h + 1) * 32] = cvtpk(ka[1], kb[1]);
            if (h == 0) {
                SBAR();
                lds_wait_ge(SWA(SW_TU), 4);
#pragma unroll
                for (int hh = 0; hh < 2; ++hh) {
                    const int ch = chl + 32 * hh;
                    const u32x4 va = *(const u32x4 LDSP*)(lu + ch * 32 + ((sg ^ ((ch >> 3) & 7)) << 2));
                    __builtin_amdgcn_raw_buffer_store_b128(va, rs, WS_XT + ((unsigned)(cb * 64 + ch) * L + tb * 64 + sg * 8) * 2, 0, SC1);
                }
                SBAR();
            }
        }
        CPF_DONE();

        lds_arrive(SWA(SW_T1));
        lds_wait_ge(SWA(SW_T1), 4);
#pragma unroll
        for (int h = 0; h < 2; ++h) {
            const int ch = chl + 32 * h;
            const u32x4 vb = *(const u32x4 LDSP*)(lkv + ch * 32 + ((sg ^ ((ch >> 3) & 7)) << 2));
            __builtin_amdgcn_raw_buffer_store_b128(vb, rs, WS_XT + D * L * 2 + ((unsigned)(cb * 64 + ch) * L + tb * 64 + sg * 8) * 2, 0, SC1);
        }

        SBAR();
        const u64 tag = (((u64)(unsigned)__builtin_amdgcn_readfirstlane(genw[1]) << 32) | (unsigned)__builtin_amdgcn_readfirstlane(genw[0])) + TAG_INC;
        unsigned gpk;
        {
            float s[2];
#pragma unroll
            for (int i = 0; i < 2; ++i) {
                float acc = 0.f;
#pragma unroll
                for (int d = 0; d < 4; ++d) {
                    const f32x2 pr = bf_pair(gx[i][d]) * bf_pair(gwv[d]);
                    acc = addbf(pr[0], pr[1], acc);
                }
                s[i] = wave_sum63(acc);
            }
            const unsigned sp = cvtpk(s[0], s[1]);
            const float e0 = __builtin_amdgcn_exp2f(-1.4426950408889634f * bf_lo(sp));
            const float e1 = __builtin_amdgcn_exp2f(-1.4426950408889634f * bf_hi(sp));
            gpk = cvtpk(__builtin_amdgcn_rcpf(1.0f + e0), __builtin_amdgcn_rcpf(1.0f + e1));
        }
        SBAR();
        asm volatile("s_waitcnt vmcnt(0)" ::: "memory");

        {
            u64 sv; unsigned r, s;
            const unsigned one = 1, fo = WS_FL + (cb * 32 + tb) * 128;
            asm volatile("s_mov_b64 %0, exec\n\t"
                         "s_mov_b64 exec, 1\n\t"
                         "ds_add_rtn_u32 %1, %3, %4\n\t"
                         "s_waitcnt lgkmcnt(0)\n\t"
                         "v_readfirstlane_b32 %2, %1\n\t"
                         "s_cmp_eq_u32 %2, 3\n\t"
                         "s_cselect_b64 exec, 1, 0\n\t"
                         "global_store_dwordx2 %5, %6, %7 sc1\n\t"
                         "s_mov_b64 exec, %0"
                         : "=&s"(sv), "=&v"(r), "=&s"(s) : "v"(SWA(SW_T2)), "v"(one), "v"(fo), "v"(tag), "s"(ws) : "memory", "scc");
        }
        if (lane == 63) __hip_atomic_store((GAS u64*)(ws + WS_G) + (gr >> 1), (u64)gpk | ((u64)(unsigned)tag << 32), RLX_AGENT);
        __builtin_amdgcn_s_setprio(0);
        const int g = lane >> 4, q = (lane >> 2) & 3, p = lane & 3;
        const char LDSP* xb = xs + 2 * (XPAD + 12 + 16 * p - 32 * g - 16 - 4 * q);
        const char LDSP* fb = fs + 2 * (FPAD - 12 + 4 * p + 32 * g + 4 * q);
        float LDSP* yf = (float LDSP*)(smem + YF_OFF + wv * YF_B) + lane;
        unsigned LDSP* prog = (unsigned LDSP*)(smem + SYNC_OFF) + SW_PROG + wv;
        const unsigned xo = WS_XT + ((unsigned)conv * D + c) * (L * 2) + lane * 16;
        {
            const unsigned fo = WS_FL + (cb * 32 + (lane & 31)) * 128;
            const u32x4 gov = {1u, 1u, (unsigned)tag, (unsigned)(tag >> 32)};
            u64 v, sv; unsigned cnt, s, w;
            asm volatile("s_mov_b32 %1, 0x100000\n\t"
                         "s_cmp_lg_u32 %9, 0\n\t"
                         "s_cbranch_scc1 2f\n\t"
                         "s_mov_b64 %2, exec\n\t"
                         "s_mov_b64 exec, 0xffffffff\n"
                         "1:\n\t"
                         "global_load_dwordx2 %0, %5, %6 sc1\n\t"
                         "s_waitcnt vmcnt(0)\n\t"
                         "v_cmp_ne_u64 vcc, %7, %0\n\t"
                         "s_sub_u32 %1, %1, 1\n\t"
                         "s_cselect_b64 vcc, 0, vcc\n\t"
                         "s_and_b64 exec, exec, vcc\n\t"
                         "s_cbranch_scc1 1b\n\t"
                         "s_mov_b64 exec, %2\n\t"
                         "ds_write_b128 %8, %10\n\t"
                         "s_branch 3f\n"
                         "2:\n\t"
                         "s_sleep 1\n\t"
                         "ds_read_b32 %4, %8\n\t"
                         "s_waitcnt lgkmcnt(0)\n\t"
                         "v_readfirstlane_b32 %3, %4\n\t"
                         "s_sub_u32 %1, %1, 1\n\t"
                         "s_cselect_b32 %3, 1, %3\n\t"
                         "s_cmp_lg_u32 %3, 0\n\t"
                         "s_cbranch_scc0 2b\n"
                         "3:"
                         : "=&v"(v), "=&s"(cnt), "=&s"(sv), "=&s"(s), "=&v"(w)
                         : "v"(fo), "s"(ws), "s"(tag), "v"(SWA(SW_GO)), "s"(wv), "v"(gov) : "memory", "scc", "vcc");
        }

        u32x4 xv[4];
#pragma unroll
        for (int i = 0; i < 4; ++i) xv[i] = __builtin_amdgcn_raw_buffer_load_b128(rs, xo + i * 1024, 0, SC1);
        lds_wait_ge(SWA(SW_FR + wv), 2);

        s16x4 xh[64], fh_[32];
        f32x4 acc[32];
        static_for<0, 32>([&](auto ic_) { constexpr int i = decltype(ic_)::value; fh_[i] = trread(fb + 256 * (i >> 1) + 32 * (i & 1)); });

#define XFRAG(T) __builtin_shufflevector(xh[2 * (T)], xh[2 * (T) + 1], 0, 1, 2, 3, 4, 5, 6, 7)
#define FFRAG(K) __builtin_shufflevector(fh_[2 * (K)], fh_[2 * (K) + 1], 0, 1, 2, 3, 4, 5, 6, 7)
        float c1 = 0.f, c2 = 0.f, c3 = 0.f, s1 = 0.f, s2 = 0.f, tm = 0.f, yv = 0.f;
        unsigned lastm = lane == 63 ? 0xffffffffu : 0u;
        PIN(lastm);
#define XREAD(H)                                                                                          \
    do {                                                                                                  \
        if constexpr (((H) & 15) == 0) *(u32x4 LDSP*)(xs + XPAD * 2 + ((H) >> 4) * 1024 + lane * 16) = xv[(H) >> 4]; \
        xh[H] = trread(xb + 128 * ((H) >> 1) + (((H) & 1) ? 0 : 32));                                     \
    } while (0)
#define BFI(d, c, v) asm volatile("v_bfi_b32 %0, %1, %2, %3" : "=v"(d) : "v"(lastm), "v"(c), "v"(v))
#define ADDROR(d, t, v, NP)                                                                               \
    do {                                                                                                  \
        if constexpr ((NP) == 0) asm volatile("v_add_f32_dpp %0, %1, %2 wave_ror:1 row_mask:0xf bank_mask:0xf" : "=v"(d) : "v"(t), "v"(v)); \
        if constexpr ((NP) == 1) asm volatile("s_nop 0\n\tv_add_f32_dpp %0, %1, %2 wave_ror:1 row_mask:0xf bank_mask:0xf" : "=v"(d) : "v"(t), "v"(v)); \
        if constexpr ((NP) == 2) asm volatile("s_nop 1\n\tv_add_f32_dpp %0, %1, %2 wave_ror:1 row_mask:0xf bank_mask:0xf" : "=v"(d) : "v"(t), "v"(v)); \
    } while (0)
#define FIN(T, W, NP)                                                                                     \
    do {                                                                                                  \
        if constexpr ((W) == 0) { BFI(tm, c3, acc[T][3]); c3 = acc[T][3]; }                               \
        if constexpr ((W) == 1) ADDROR(s2, tm, acc[T][2], NP);                                            \
        if constexpr ((W) == 2) { BFI(tm, c2, s2); c2 = s2; }                                             \
        if constexpr ((W) == 3) ADDROR(s1, tm, acc[T][1], NP);                                            \
        if constexpr ((W) == 4) { BFI(tm, c1, s1); c1 = s1; }                                             \
        if constexpr ((W) == 5) ADDROR(yv, tm, acc[T][0], NP);                                            \
        if constexpr ((W) == 6) yf[(T) * 64] = yv;                                                        \
        if constexpr ((W) == 7) { CBAR(); __hip_atomic_store(prog, (unsigned)((T) + 1), RLX_WG); }        \
    } while (0)
        static_for<0, 16>([&](auto hc) { constexpr int h = decltype(hc)::value; XREAD(h); });
        SBAR();

        static_for<0, 32>([&](auto phc) {
            constexpr int ph = decltype(phc)::value;
            constexpr int n = (ph >> 1) + 1;
            f32x4 ac;
            static_for<0, n>([&](auto kpc) {
                constexpr int kp = decltype(kpc)::value;
                constexpr int k2 = n - 1 - kp;
                constexpr int m = tile_start(ph) + kp;
                const s16x8 av = XFRAG(ph - 2 * k2), bv = FFRAG(k2);
                if constexpr (kp == 0) asm volatile("v_mfma_f32_16x16x32_bf16 %0, %1, %2, 0" : "=&v"(ac) : "v"(av), "v"(bv));
                else asm volatile("v_mfma_f32_16x16x32_bf16 %0, %1, %2, %0" : "+v"(ac) : "v"(av), "v"(bv));
                if constexpr (kp == n - 1) acc[ph] = ac;
                SBAR();
                if constexpr (SCHED.xr[m] >= 0) { XREAD(SCHED.xr[m]); SBAR(); }
                if constexpr (SCHED.fin[m] >= 0) FIN(SCHED.fin[m] >> 3, SCHED.fin[m] & 7, (SCHED.xr[m] >= 0 ? 0 : 1));
                SBAR();
            });
        });
        asm volatile("s_nop 7\n\ts_nop 1" ::: "memory");
        static_for<NMFMA, SCHED.nslot>([&](auto mc) { constexpr int m = decltype(mc)::value; FIN(SCHED.fin[m] >> 3, SCHED.fin[m] & 7, 2); SBAR(); });
    }
}

extern "C" void kernel_launch(void* const* d_in, const int* in_sizes, int n_in, void* d_out, int out_size, void* d_ws,
                              size_t ws_size, hipStream_t stream) {
    Args a;
    a.u = (const u16*)d_in[0];
    a.Are = (const u16*)d_in[1];
    a.Aim = (const u16*)d_in[2];
    a.Bre = (const u16*)d_in[3];
    a.Bim = (const u16*)d_in[4];
    a.Cre = (const u16*)d_in[5];
    a.Cim = (const u16*)d_in[6];
    a.logdt = (const u16*)d_in[7];
    a.dskip = (const u16*)d_in[8];
    a.kk = (const u16*)d_in[10];
    a.vv = (const u16*)d_in[11];
    a.shk = (const u16*)d_in[12];
    a.ssmk = (const u16*)d_in[13];
    a.ssmd = (const u16*)d_in[14];
    a.gw = (const u16*)d_in[15];
    a.out = (u16*)d_out;
    a.ws = (char*)d_ws;
    (void)hipFuncSetAttribute((const void*)fused_kernel, hipFuncAttributeMaxDynamicSharedMemorySize, LDS_BYTES);
    fused_kernel<<<256, 512, LDS_BYTES, stream>>>(a);
}
```
